```python
import torch

def _softmax(x, axis=-1):
    return torch.softmax(x, dim=axis)
def _gelu(x):
    return torch.nn.functional.gelu(x, approximate="tanh")
def _silu(x):
    return torch.nn.functional.silu(x)
def _sigmoid(x):
    return torch.sigmoid(x)
def _layernorm(x, axis=-1, eps=1e-5):
    m = x.mean(dim=axis, keepdim=True)
    v = x.var(dim=axis, keepdim=True, unbiased=False)
    return (x - m) / torch.sqrt(v + eps)
def _scatter_add(tgt, idx, src, axis=0):
    return tgt.clone().index_add_(axis, idx, src)
def _segment_sum(src, seg, n):
    out = torch.zeros((n,) + src.shape[1:], dtype=src.dtype)
    return out.index_add_(0, seg, src)
def _pad(x, pw):
    flat = tuple(v for pair in reversed(pw) for v in pair)
    return torch.nn.functional.pad(x, flat)

kernel_name = 'concat_heads__gqa_block_cached__gqa_block_cached__near_ridge'

def setup_inputs():

    _w = (lambda a: (lambda t: t.to(torch.bfloat16) if t.is_floating_point() else t)(torch.as_tensor(a)))
    def _A_setup():
        B, Sq, D = 8, 64, 448
        Hq, Hkv, Sk, Dh = 16, 8, 512, 256
        assert Hq % Hkv == 0, (Hq, Hkv)
        return {
            "x": _w((lambda *s: torch.randn(*s, dtype=torch.bfloat16) * 0.8)(B, Sq, D)),
            "k_cache": _w((lambda *s: torch.randn(*s, dtype=torch.bfloat16) * 0.8)(B, Hkv, Sk, Dh)),
            "v_cache": _w((lambda *s: torch.randn(*s, dtype=torch.bfloat16) * 0.05)(B, Hkv, Sk, Dh)),
            "wq": _w((lambda *s: torch.randn(*s, dtype=torch.bfloat16) * 0.8)(D, Hq * Dh) * (D ** -0.5)),
            "wo": _w((lambda *s: torch.randn(*s, dtype=torch.bfloat16) * 0.05)(Hq * Dh, D) * ((Hq * Dh) ** -0.5)),
        }
    _A_in = _A_setup()

    def _B_setup():
        B, Sq, D = 8, 64, 448
        Hq, Hkv, Sk, Dh = 16, 8, 512, 256
        assert Hq % Hkv == 0, (Hq, Hkv)
        return {
            "x": _w((lambda *s: torch.randn(*s, dtype=torch.bfloat16) * 0.8)(B, Sq, D)),
            "k_cache": _w((lambda *s: torch.randn(*s, dtype=torch.bfloat16) * 0.8)(B, Hkv, Sk, Dh)),
            "v_cache": _w((lambda *s: torch.randn(*s, dtype=torch.bfloat16) * 0.05)(B, Hkv, Sk, Dh)),
            "wq": _w((lambda *s: torch.randn(*s, dtype=torch.bfloat16) * 0.8)(D, Hq * Dh) * (D ** -0.5)),
            "wo": _w((lambda *s: torch.randn(*s, dtype=torch.bfloat16) * 0.05)(Hq * Dh, D) * ((Hq * Dh) ** -0.5)),
        }
    _B_in = _B_setup()

    return {
        "A_x": _A_in["x"],
        "A_k_cache": _A_in["k_cache"],
        "A_v_cache": _A_in["v_cache"],
        "A_wq": _A_in["wq"],
        "A_wo": _A_in["wo"],
        "B_x": _B_in["x"],
        "B_k_cache": _B_in["k_cache"],
        "B_v_cache": _B_in["v_cache"],
        "B_wq": _B_in["wq"],
        "B_wo": _B_in["wo"],
    }

def reference(A_x, A_k_cache, A_v_cache, A_wq, A_wo, B_x, B_k_cache, B_v_cache, B_wq, B_wo):
    def _A_ref(x, k_cache, v_cache, wq, wo):
        B, Sq, D = x.shape
        Hq, Dh = 16, 256
        Hkv = k_cache.shape[1]
        rep = Hq // Hkv
        q = (lambda x, *d: x.permute(*d))((x @ wq).reshape(B, Sq, Hq, Dh), 0, 2, 1, 3)
        k = (lambda x, n, axis: torch.repeat_interleave(x, n, dim=axis))(k_cache, rep, axis=1)
        v = (lambda x, n, axis: torch.repeat_interleave(x, n, dim=axis))(v_cache, rep, axis=1)
        scores = torch.einsum("bhqd,bhkd->bhqk", q, k) * (Dh ** -0.5)
        p = _softmax(scores, axis=-1)
        o = torch.einsum("bhqk,bhkd->bhqd", p, v)
        return (lambda x, *d: x.permute(*d))(o, 0, 2, 1, 3).reshape(B, Sq, Hq * Dh) @ wo

    def _B_ref(x, k_cache, v_cache, wq, wo):
        B, Sq, D = x.shape
        Hq, Dh = 16, 256
        Hkv = k_cache.shape[1]
        rep = Hq // Hkv
        q = (lambda x, *d: x.permute(*d))((x @ wq).reshape(B, Sq, Hq, Dh), 0, 2, 1, 3)
        k = (lambda x, n, axis: torch.repeat_interleave(x, n, dim=axis))(k_cache, rep, axis=1)
        v = (lambda x, n, axis: torch.repeat_interleave(x, n, dim=axis))(v_cache, rep, axis=1)
        scores = torch.einsum("bhqd,bhkd->bhqk", q, k) * (Dh ** -0.5)
        p = _softmax(scores, axis=-1)
        o = torch.einsum("bhqk,bhkd->bhqd", p, v)
        return (lambda x, *d: x.permute(*d))(o, 0, 2, 1, 3).reshape(B, Sq, Hq * Dh) @ wo

    a = _A_ref(A_x, A_k_cache, A_v_cache, A_wq, A_wo)
    b = _B_ref(B_x, B_k_cache, B_v_cache, B_wq, B_wo)
    return torch.concatenate([a, b], axis=-1)
```

```cpp
#include <hip/hip_runtime.h>
#include <hip/hip_bf16.h>
#pragma clang diagnostic ignored "-Wint-to-pointer-cast"

typedef __attribute__((ext_vector_type(8))) __bf16 bf16x8;
typedef __attribute__((ext_vector_type(4))) __bf16 bf16x4;
typedef __attribute__((ext_vector_type(2))) __bf16 bf16x2;
typedef __attribute__((ext_vector_type(4))) float f32x4;
typedef __attribute__((ext_vector_type(2))) float f32x2;
typedef __attribute__((ext_vector_type(4))) unsigned u32x4;
typedef __attribute__((ext_vector_type(2))) unsigned u32x2;
typedef __attribute__((address_space(1))) unsigned gu32;
#define LAS __attribute__((address_space(3)))
#define RLX_AGENT __ATOMIC_RELAXED, __HIP_MEMORY_SCOPE_AGENT

#define DIAG 0

constexpr unsigned SLOT = 32768u;
constexpr unsigned AUX = 4u * SLOT;
constexpr unsigned STATS = SLOT + 28672u;
constexpr unsigned PART_BYTES = 57344u;
constexpr unsigned FLAG_OFF = 256u * PART_BYTES;
constexpr unsigned FLAG_STRIDE = 256u;
constexpr unsigned DIAG_OFF = FLAG_OFF + 256u * FLAG_STRIDE;

#define WAIT_V(n) asm volatile("s_waitcnt vmcnt(" #n ")" ::: "memory")
#define WAIT_L0() asm volatile("s_waitcnt lgkmcnt(0)" ::: "memory")
#define BAR() asm volatile("s_barrier" ::: "memory")

__device__ __forceinline__ unsigned pack2(float a, float b) {
  f32x2 v = {a, b};
  bf16x2 p = __builtin_convertvector(v, bf16x2);
  return __builtin_bit_cast(unsigned, p);
}

__device__ __forceinline__ void dma4(unsigned lds, unsigned va, unsigned vb, const char* g0, const char* g1) {
  unsigned keep;
  asm volatile(
      "s_nop 4\n\t"
      "s_mov_b32 %0, m0\n\t"
      "s_mov_b32 m0, %1\n\t"
      "s_nop 0\n\t"
      "global_load_lds_dwordx4 %2, %4\n\t"
      "s_add_u32 m0, m0, 0x400\n\t"
      "s_nop 0\n\t"
      "global_load_lds_dwordx4 %3, %4\n\t"
      "s_add_u32 m0, m0, 0x400\n\t"
      "s_nop 0\n\t"
      "global_load_lds_dwordx4 %2, %5\n\t"
      "s_add_u32 m0, m0, 0x400\n\t"
      "s_nop 0\n\t"
      "global_load_lds_dwordx4 %3, %5\n\t"
      "s_mov_b32 m0, %0"
      : "=&s"(keep)
      : "s"(lds), "v"(va), "v"(vb), "s"(g0), "s"(g1)
      : "memory", "scc");
}
__device__ __forceinline__ void dma1(unsigned lds, unsigned va, const char* g0) {
  unsigned keep;
  asm volatile(
      "s_nop 4\n\t"
      "s_mov_b32 %0, m0\n\t"
      "s_mov_b32 m0, %1\n\t"
      "s_nop 0\n\t"
      "global_load_lds_dwordx4 %2, %3\n\t"
      "s_mov_b32 m0, %0"
      : "=&s"(keep)
      : "s"(lds), "v"(va), "s"(g0)
      : "memory");
}
__device__ __forceinline__ void dma_wo(unsigned lds, unsigned va, unsigned vb, const char* g0) {
  unsigned keep;
  unsigned long long ex;
  asm volatile(
      "s_nop 4\n\t"
      "s_mov_b32 %0, m0\n\t"
      "s_mov_b64 %1, exec\n\t"
      "s_mov_b32 m0, %2\n\t"
      "s_mov_b32 exec_hi, 0xffffff\n\t"
      "s_nop 0\n\t"
      "global_load_lds_dwordx4 %3, %5\n\t"
      "global_load_lds_dwordx4 %3, %5 offset:896\n\t"
      "global_load_lds_dwordx4 %4, %5 offset:1792\n\t"
      "global_load_lds_dwordx4 %4, %5 offset:2688\n\t"
      "s_mov_b64 exec, %1\n\t"
      "s_mov_b32 m0, %0"
      : "=&s"(keep), "=&s"(ex)
      : "s"(lds), "v"(va), "v"(vb), "s"(g0)
      : "memory");
}

template <int OFF>
__device__ __forceinline__ void kload(u32x4& dst, unsigned voff, const char* base) {
  asm volatile("global_load_dwordx4 %0, %1, %2 offset:%3" : "=v"(dst) : "v"(voff), "s"(base), "i"(OFF) : "memory");
}

__device__ __forceinline__ bf16x8 lds_b128(unsigned addr) { return *(LAS bf16x8*)(addr); }
__device__ __forceinline__ bf16x8 lds_tr(unsigned a0, unsigned a1) {
  bf16x4 lo = __builtin_amdgcn_ds_read_tr16_b64_v4bf16((LAS bf16x4*)(a0));
  bf16x4 hi = __builtin_amdgcn_ds_read_tr16_b64_v4bf16((LAS bf16x4*)(a1));
  return __builtin_shufflevector(lo, hi, 0, 1, 2, 3, 4, 5, 6, 7);
}
__device__ __forceinline__ f32x4 mfma(bf16x8 a, bf16x8 b, f32x4 c) {
  return __builtin_amdgcn_mfma_f32_16x16x32_bf16(a, b, c, 0, 0, 0);
}
__device__ __forceinline__ float xg_max(float v) {
  auto a = __builtin_amdgcn_permlane16_swap(__float_as_uint(v), __float_as_uint(v), false, false);
  v = fmaxf(__uint_as_float(a[0]), __uint_as_float(a[1]));
  auto b = __builtin_amdgcn_permlane32_swap(__float_as_uint(v), __float_as_uint(v), false, false);
  return fmaxf(__uint_as_float(b[0]), __uint_as_float(b[1]));
}
__device__ __forceinline__ float xg_sum(float v) {
  auto a = __builtin_amdgcn_permlane16_swap(__float_as_uint(v), __float_as_uint(v), false, false);
  v = __uint_as_float(a[0]) + __uint_as_float(a[1]);
  auto b = __builtin_amdgcn_permlane32_swap(__float_as_uint(v), __float_as_uint(v), false, false);
  return __uint_as_float(b[0]) + __uint_as_float(b[1]);
}

#define STAMP(i) do { } while (0)

__global__ __launch_bounds__(512, 2) void gqa2_fused(
    const char* __restrict__ xA, const char* __restrict__ kA, const char* __restrict__ vA, const char* __restrict__ wqA,
    const char* __restrict__ woA, const char* __restrict__ xB, const char* __restrict__ kB, const char* __restrict__ vB,
    const char* __restrict__ wqB, const char* __restrict__ woB, char* __restrict__ out, char* ws) {
  __shared__ __attribute__((aligned(16))) char smem[163840];
  const unsigned L0 = (unsigned)(size_t)(LAS char*)smem;

  const unsigned tid = threadIdx.x;
  const unsigned w = __builtin_amdgcn_readfirstlane(tid >> 6);
  const unsigned l = tid & 63, r = l & 15, g = l >> 4, qq = (l & 15) >> 2, p = l & 3;

  const unsigned bid = blockIdx.x, xcd = bid & 7, idx = bid >> 3;
  const unsigned br = xcd >> 2, b = idx & 7, h = (xcd & 3) * 4 + (idx >> 3), kvh = h >> 1;
  const unsigned rot = kvh & 3;
  const unsigned grp = br * 8 + b;

  const char* xg = (br ? xB : xA) + (size_t)b * (64 * 896);
  const char* wq = (br ? wqB : wqA) + h * 512;
  const char* kg = (br ? kB : kA) + (size_t)(b * 8 + kvh) * (512 * 512);
  const char* vg = (br ? vB : vA) + (size_t)(b * 8 + kvh) * (512 * 512);
  const char* wo = (br ? woB : woA) + (size_t)h * (256 * 896);

  gu32* myflags = (gu32*)(ws + FLAG_OFF + (grp * 16 + h) * FLAG_STRIDE);
  if (tid < 16) __hip_atomic_store(myflags + tid, 0u, RLX_AGENT);

  const unsigned wqf = (l >> 5) | ((w & 1) << 2);
  const unsigned vWqA = (l >> 5) * 8192 + 16 * ((l & 31) ^ (wqf << 1));
  const unsigned vWqB = (2 + (l >> 5)) * 8192 + 16 * ((l & 31) ^ ((wqf | 2) << 1));
  const unsigned vX = (l >> 3) * 896 + 16 * ((l & 7) ^ ((4 * w + (l >> 4)) & 7));
  const char* wqw = wq + (size_t)w * (8 * 8192);
  const char* xw = xg + w * (8 * 896);
#define P1_STAGE(s)                                                                                   \
  do {                                                                                                \
    dma4(L0 + ((s) & 3) * SLOT + w * 4096, vWqA, vWqB, wqw + (size_t)(s) * (64 * 8192),               \
         wqw + (size_t)(s) * (64 * 8192) + 4 * 8192);                                                 \
    dma1(L0 + AUX + ((s) & 3) * 8192 + w * 1024, vX, xw + (s) * 128);                                 \
  } while (0)

  P1_STAGE(0);
  P1_STAGE(1);
  P1_STAGE(2);
  P1_STAGE(3);

  u32x4 kf[4][8];
  const unsigned vK = (8 * (r >> 2) + (r & 3)) * 512 + g * 16;
  const char* kw0 = kg + w * (64 * 512);
  const char* kw1 = kw0 + 16384;
#define K_GROUP(m)                                            \
  do {                                                        \
    kload<128 * (m)>(kf[0][2 * (m)], vK, kw0);                \
    kload<128 * (m) + 64>(kf[0][2 * (m) + 1], vK, kw0);       \
    kload<128 * (m) + 2048>(kf[1][2 * (m)], vK, kw0);         \
    kload<128 * (m) + 2112>(kf[1][2 * (m) + 1], vK, kw0);     \
    kload<128 * (m)>(kf[2][2 * (m)], vK, kw1);                \
    kload<128 * (m) + 64>(kf[2][2 * (m) + 1], vK, kw1);       \
    kload<128 * (m) + 2048>(kf[3][2 * (m)], vK, kw1);         \
    kload<128 * (m) + 2112>(kf[3][2 * (m) + 1], vK, kw1);     \
  } while (0)
  K_GROUP(0);

  const unsigned vVA = (l >> 3) * 512 + 16 * ((l & 7) ^ (((l >> 4) & 1) << 1));
  const unsigned vVB = 4096 + (l >> 3) * 512 + 16 * ((l & 7) ^ ((((l >> 4) & 1) | 2) << 1));
  const unsigned vWoA = 16 * (l ^ ((w & 2) << 1));
  const unsigned vWoB = 16 * (l ^ (((w & 2) | 1) << 1));
  const char* vw = vg + w * (32 * 512);
  const char* wow = wo + w * (4 * 896);
#define P3_STAGE(t)                                                                                   \
  do {                                                                                                \
    const unsigned cc_ = (((t) >> 2) + rot) & 3;                                                      \
    constexpr unsigned sl_ = (((t) + 3) & 3) * SLOT;                                                  \
    if (((t) & 2) == 0) {                                                                             \
      const char* gb_ = vw + ((t) & 1) * (256 * 512) + cc_ * 128;                                     \
      dma4(L0 + sl_ + w * 4096, vVA, vVB, gb_, gb_ + 8192);                                           \
    } else {                                                                                          \
      dma_wo(L0 + sl_ + w * 3584, vWoA, vWoB, wow + (cc_ * 64 + ((t) & 1) * 32) * 896);               \
    }                                                                                                 \
  } while (0)

  f32x4 qacc[2][4];
#pragma unroll
  for (int i = 0; i < 2; ++i)
#pragma unroll
    for (int j = 0; j < 4; ++j) qacc[i][j] = (f32x4){0.f, 0.f, 0.f, 0.f};

  const unsigned aWq0 = L0 + (8 * g + qq) * 512 + (((2 * w) ^ (qq | ((g & 1) << 2))) * 32) + 8 * p;
  const unsigned aX0 = L0 + AUX + r * 128 + 16 * (g ^ (r >> 1));
#define P1_COMPUTE(s)                                                                                 \
  do {                                                                                                \
    _Pragma("unroll") for (int u = 0; u < 2; ++u) {                                                   \
      bf16x8 xb[4], wa[2];                                                                            \
      _Pragma("unroll") for (int qt = 0; qt < 4; ++qt)                                                \
          xb[qt] = lds_b128((aX0 ^ (u * 64)) + ((s) & 3) * 8192 + qt * 2048);                         \
      _Pragma("unroll") for (int dt = 0; dt < 2; ++dt) {                                              \
        const unsigned a_ = (aWq0 ^ (dt * 32)) + ((s) & 3) * SLOT + u * 16384;                        \
        wa[dt] = lds_tr(a_, a_ + 2048);                                                               \
      }                                                                                               \
      _Pragma("unroll") for (int dt = 0; dt < 2; ++dt)                                                \
          _Pragma("unroll") for (int qt = 0; qt < 4; ++qt) qacc[dt][qt] = mfma(wa[dt], xb[qt], qacc[dt][qt]); \
    }                                                                                                 \
  } while (0)

  WAIT_V(23); BAR(); P1_COMPUTE(0);
  STAMP(1);
  WAIT_L0(); WAIT_V(18); BAR(); P1_STAGE(4); P1_COMPUTE(1);
  WAIT_L0(); WAIT_V(18); BAR(); P1_STAGE(5); P1_COMPUTE(2);
  WAIT_L0(); WAIT_V(18); BAR(); P1_STAGE(6); K_GROUP(1); K_GROUP(2); K_GROUP(3); P1_COMPUTE(3);
  WAIT_L0(); WAIT_V(34); BAR(); P3_STAGE(0); P1_COMPUTE(4);
  WAIT_L0(); WAIT_V(33); BAR(); P3_STAGE(1); P1_COMPUTE(5);
  WAIT_L0(); WAIT_V(32); BAR(); P3_STAGE(2); P1_COMPUTE(6);
  WAIT_L0(); BAR();

#pragma unroll
  for (int dt = 0; dt < 2; ++dt)
#pragma unroll
    for (int qt = 0; qt < 4; ++qt) {
      u32x2 v = {pack2(qacc[dt][qt][0], qacc[dt][qt][1]), pack2(qacc[dt][qt][2], qacc[dt][qt][3])};
      *(LAS u32x2*)(L0 + 2 * SLOT + (w * 4 + qt) * 1024 + ((2 * dt + (g >> 1)) * 16 + r) * 16 + 8 * (g & 1)) = v;
    }
  WAIT_L0(); BAR();

  f32x4 sacc[4][4];
#pragma unroll
  for (int i = 0; i < 4; ++i)
#pragma unroll
    for (int j = 0; j < 4; ++j) sacc[i][j] = (f32x4){0.f, 0.f, 0.f, 0.f};
  const unsigned aQ = L0 + 2 * SLOT + l * 16;
#define K_PIN(m)                                                                                      \
  "+v"(kf[0][2 * (m)]), "+v"(kf[0][2 * (m) + 1]), "+v"(kf[1][2 * (m)]), "+v"(kf[1][2 * (m) + 1]),     \
      "+v"(kf[2][2 * (m)]), "+v"(kf[2][2 * (m) + 1]), "+v"(kf[3][2 * (m)]), "+v"(kf[3][2 * (m) + 1])
#define S_COMPUTE(m, n)                                                                               \
  do {                                                                                                \
    asm volatile("s_waitcnt vmcnt(" #n ")" : K_PIN(m) : : "memory");                                  \
    _Pragma("unroll") for (int ks = 2 * (m); ks < 2 * (m) + 2; ++ks) {                                \
      bf16x8 qb[4];                                                                                   \
      _Pragma("unroll") for (int qt = 0; qt < 4; ++qt) qb[qt] = lds_b128(aQ + (ks * 4 + qt) * 1024);  \
      _Pragma("unroll") for (int kt = 0; kt < 4; ++kt)                                                \
          _Pragma("unroll") for (int qt = 0; qt < 4; ++qt)                                            \
              sacc[kt][qt] = mfma(__builtin_bit_cast(bf16x8, kf[kt][ks]), qb[qt], sacc[kt][qt]);      \
    }                                                                                                 \
  } while (0)
  S_COMPUTE(0, 36);
  S_COMPUTE(1, 28);
  S_COMPUTE(2, 20);
  S_COMPUTE(3, 12);

  float mx[4];
#pragma unroll
  for (int qt = 0; qt < 4; ++qt) {
    float m = sacc[0][qt][0];
#pragma unroll
    for (int kt = 0; kt < 4; ++kt)
#pragma unroll
      for (int i = 0; i < 4; ++i) m = fmaxf(m, sacc[kt][qt][i]);
    mx[qt] = xg_max(m);
  }
  {
    const float mine = g == 0 ? mx[0] : g == 1 ? mx[1] : g == 2 ? mx[2] : mx[3];
    *(LAS float*)(L0 + STATS + l * 32 + w * 4) = mine;
  }
  WAIT_L0(); BAR();
  constexpr float CEXP = 1.4426950408889634f / 16.0f;
  float rs[4];
#pragma unroll
  for (int qt = 0; qt < 4; ++qt) {
    const f32x4 m0 = *(LAS f32x4*)(L0 + STATS + (16 * qt + r) * 32);
    const f32x4 m1 = *(LAS f32x4*)(L0 + STATS + (16 * qt + r) * 32 + 16);
    const float m = fmaxf(fmaxf(fmaxf(m0[0], m0[1]), fmaxf(m0[2], m0[3])), fmaxf(fmaxf(m1[0], m1[1]), fmaxf(m1[2], m1[3])));
    const float mc = -m * CEXP;
    float s = 0.f;
#pragma unroll
    for (int kt = 0; kt < 4; ++kt)
#pragma unroll
      for (int i = 0; i < 4; ++i) {
        const float e = __builtin_amdgcn_exp2f(fmaf(sacc[kt][qt][i], CEXP, mc));
        sacc[kt][qt][i] = e;
        s += e;
      }
    rs[qt] = xg_sum(s);
#pragma unroll
    for (int u = 0; u < 2; ++u) {
      u32x4 v = {pack2(sacc[2 * u][qt][0], sacc[2 * u][qt][1]), pack2(sacc[2 * u][qt][2], sacc[2 * u][qt][3]),
                 pack2(sacc[2 * u + 1][qt][0], sacc[2 * u + 1][qt][1]), pack2(sacc[2 * u + 1][qt][2], sacc[2 * u + 1][qt][3])};
      *(LAS u32x4*)(L0 + (qt < 2 ? 2 * SLOT : AUX) + ((qt & 1) * 16 + 2 * w + u) * 1024 + l * 16) = v;
    }
  }
  {
    const float mine = g == 0 ? rs[0] : g == 1 ? rs[1] : g == 2 ? rs[2] : rs[3];
    *(LAS float*)(L0 + STATS + 2048 + l * 32 + w * 4) = mine;
  }
  WAIT_L0(); BAR();

  const unsigned qh = w >> 2, dq = w & 3;
  bf16x8 pf[2][16];
  {
    const unsigned aP = L0 + (qh ? AUX : 2 * SLOT) + l * 16;
#pragma unroll
    for (int qt = 0; qt < 2; ++qt)
#pragma unroll
      for (int ks = 0; ks < 16; ++ks) pf[qt][ks] = lds_b128(aP + (qt * 16 + ks) * 1024);
  }
  float linv[2];
#pragma unroll
  for (int qt = 0; qt < 2; ++qt) {
    const f32x4 s0 = *(LAS f32x4*)(L0 + STATS + 2048 + (32 * qh + 16 * qt + r) * 32);
    const f32x4 s1 = *(LAS f32x4*)(L0 + STATS + 2048 + (32 * qh + 16 * qt + r) * 32 + 16);
    linv[qt] = 1.0f / (((s0[0] + s0[1]) + (s0[2] + s0[3])) + ((s1[0] + s1[1]) + (s1[2] + s1[3])));
  }
  f32x4 acc[7][2];
#pragma unroll
  for (int j = 0; j < 7; ++j) acc[j][0] = acc[j][1] = (f32x4){0.f, 0.f, 0.f, 0.f};
  f32x4 oacc[2];
  const unsigned fsw = ((qq >> 1) & 1) | ((g & 1) << 1);
  const unsigned aV = L0 + (8 * g + qq) * 128 + ((dq ^ fsw) * 32) + 8 * p;
  const unsigned aWo = L0 + (8 * g + qq) * 896 + ((dq ^ fsw) * 32) + 8 * p;
  const unsigned aOw = L0 + AUX + ((dq >> 1) * 4 + 2 * qh) * 1024 + ((2 * (dq & 1) + (g >> 1)) * 16 + r) * 16 + 8 * (g & 1);
  const unsigned aOr = L0 + AUX + (2 * qh) * 1024 + l * 16;

#define P3_COMPUTE(t)                                                                                 \
  do {                                                                                                \
    constexpr unsigned sl_ = (((t) + 3) & 3) * SLOT;                                                  \
    if (((t) & 2) == 0) {                                                                             \
      if (((t) & 1) == 0) oacc[0] = oacc[1] = (f32x4){0.f, 0.f, 0.f, 0.f};                            \
      _Pragma("unroll") for (int u = 0; u < 8; ++u) {                                                 \
        const unsigned a_ = aV + sl_ + u * 4096;                                                      \
        const bf16x8 va = lds_tr(a_, a_ + 512);                                                       \
        oacc[0] = mfma(va, pf[0][((t) & 1) * 8 + u], oacc[0]);                                        \
        oacc[1] = mfma(va, pf[1][((t) & 1) * 8 + u], oacc[1]);                                        \
      }                                                                                               \
      if (((t) & 1) == 1) {                                                                           \
        _Pragma("unroll") for (int qt = 0; qt < 2; ++qt) {                                            \
          u32x2 v = {pack2(oacc[qt][0] * linv[qt], oacc[qt][1] * linv[qt]),                           \
                     pack2(oacc[qt][2] * linv[qt], oacc[qt][3] * linv[qt])};                          \
          *(LAS u32x2*)(aOw + qt * 1024) = v;                                                         \
        }                                                                                             \
      }                                                                                               \
    } else {                                                                                          \
      const bf16x8 ob0 = lds_b128(aOr + ((t) & 1) * 4096);                                            \
      const bf16x8 ob1 = lds_b128(aOr + ((t) & 1) * 4096 + 1024);                                     \
      _Pragma("unroll") for (int j = 0; j < 7; ++j) {                                                 \
        const unsigned a_ = aWo + sl_ + j * 128;                                                      \
        const bf16x8 wa = lds_tr(a_, a_ + 3584);                                                      \
        acc[j][0] = mfma(wa, ob0, acc[j][0]);                                                         \
        acc[j][1] = mfma(wa, ob1, acc[j][1]);                                                         \
      }                                                                                               \
    }                                                                                                 \
  } while (0)
#define P3_STEP(t)                                                                                    \
  do {                                                                                                \
    WAIT_L0(); WAIT_V(8); BAR();                                                                      \
    P3_STAGE((t) + 3);                                                                                \
    P3_COMPUTE(t);                                                                                    \
  } while (0)

  P3_STEP(0);  P3_STEP(1);  P3_STEP(2);  P3_STEP(3);
  P3_STEP(4);  P3_STEP(5);  P3_STEP(6);  P3_STEP(7);
  P3_STEP(8);  P3_STEP(9);  P3_STEP(10); P3_STEP(11);
  P3_STEP(12);
  WAIT_L0(); WAIT_V(8); BAR(); P3_COMPUTE(13);
  WAIT_L0(); WAIT_V(4); BAR(); P3_COMPUTE(14);
  WAIT_L0(); WAIT_V(0); BAR(); P3_COMPUTE(15);

  const __amdgpu_buffer_rsrc_t prs = __builtin_amdgcn_make_buffer_rsrc(ws + (size_t)grp * (16 * PART_BYTES), 0, 16 * PART_BYTES, 0x00020000);
  {
    const unsigned off = h * PART_BYTES + (qh * 8 + (r >> 1)) * 3584 + dq * 128 + g * 32 + (r & 1) * 16;
#pragma unroll
    for (int j = 0; j < 7; ++j) {
      u32x4 v = {pack2(acc[j][0][0], acc[j][0][1]), pack2(acc[j][0][2], acc[j][0][3]),
                 pack2(acc[j][1][0], acc[j][1][1]), pack2(acc[j][1][2], acc[j][1][3])};
      __builtin_amdgcn_raw_buffer_store_b128(v, prs, off + j * 512, 0, 16);
    }
  }
  WAIT_V(0); BAR();
  if (w == 0) {
    gu32* peer = (gu32*)(ws + FLAG_OFF + (grp * 16 + (l & 15)) * FLAG_STRIDE) + h;
    unsigned spins = 0;
    for (;;) {
      if ((spins & 63) == 0 && l < 16) __hip_atomic_store(peer, 1u, RLX_AGENT);
      const unsigned f = __hip_atomic_load(myflags + (l & 15), RLX_AGENT);
      if (__all(f == 1u)) break;
      if (++spins > (1u << 22)) break;
      __builtin_amdgcn_s_sleep(1);
    }
    if (l < 16) __hip_atomic_store(peer, 1u, RLX_AGENT);
  }
  BAR();
  __builtin_amdgcn_fence(__ATOMIC_ACQUIRE, "wavefront");
  if (w < 7) {
    const unsigned half = l >> 5, it = l & 31;
    const unsigned off = half * (8 * PART_BYTES) + h * 3584 + w * 512 + it * 16;
    u32x4 pv[8];
#pragma unroll
    for (int e = 0; e < 8; ++e) pv[e] = __builtin_amdgcn_raw_buffer_load_b128(prs, off + e * PART_BYTES, 0, 16);
    float s[8];
#pragma unroll
    for (int i = 0; i < 8; ++i) s[i] = 0.f;
#pragma unroll
    for (int e = 0; e < 8; ++e)
#pragma unroll
      for (int i = 0; i < 4; ++i) {
        s[2 * i] += __uint_as_float(pv[e][i] << 16);
        s[2 * i + 1] += __uint_as_float(pv[e][i] & 0xffff0000u);
      }
    float o[4];
#pragma unroll
    for (int i = 0; i < 4; ++i) {
      auto sw = __builtin_amdgcn_permlane32_swap(__float_as_uint(s[i]), __float_as_uint(s[4 + i]), false, false);
      o[i] = __uint_as_float(sw[0]) + __uint_as_float(sw[1]);
    }
    const unsigned q = 32 * (h >> 3) + 16 * half + 2 * (h & 7) + (it & 1);
    const unsigned n = 64 * w + 4 * (it >> 1);
    u32x2 v = {pack2(o[0], o[1]), pack2(o[2], o[3])};
    *(u32x2*)(out + (size_t)(b * 64 + q) * 1792 + br * 896 + n * 2) = v;
  }
}

extern "C" void kernel_launch(void* const* d_in, const int* in_sizes, int n_in, void* d_out, int out_size, void* d_ws,
                              size_t ws_size, hipStream_t stream) {
  gqa2_fused<<<256, 512, 0, stream>>>((const char*)d_in[0], (const char*)d_in[1], (const char*)d_in[2], (const char*)d_in[3],
                                      (const char*)d_in[4], (const char*)d_in[5], (const char*)d_in[6], (const char*)d_in[7],
                                      (const char*)d_in[8], (const char*)d_in[9], (char*)d_out, (char*)d_ws);
}
```

```cpp
#include <hip/hip_runtime.h>
#include <hip/hip_bf16.h>
#pragma clang diagnostic ignored "-Wint-to-pointer-cast"

typedef __attribute__((ext_vector_type(8))) __bf16 bf16x8;
typedef __attribute__((ext_vector_type(4))) __bf16 bf16x4;
typedef __attribute__((ext_vector_type(2))) __bf16 bf16x2;
typedef __attribute__((ext_vector_type(4))) float f32x4;
typedef __attribute__((ext_vector_type(2))) float f32x2;
typedef __attribute__((ext_vector_type(4))) unsigned u32x4;
typedef __attribute__((ext_vector_type(2))) unsigned u32x2;
typedef __attribute__((address_space(1))) unsigned gu32;
#define LAS __attribute__((address_space(3)))
#define RLX_AGENT __ATOMIC_RELAXED, __HIP_MEMORY_SCOPE_AGENT

#define DIAG 0

constexpr unsigned SLOT = 32768u;
constexpr unsigned AUX = 4u * SLOT;
constexpr unsigned PBUF = 3u * SLOT;
#define KV_NT true
constexpr unsigned slot_of(int i) {
  return i < 15 ? (i & 3) : i < 22 ? (i - 15) % 3 : i == 22 ? 3 : i == 23 ? 1 : i == 24 ? 2 : i == 25 ? 0 : i == 26 ? 3 : i == 27 ? 1 : i == 28 ? 2 : 0;
}
constexpr unsigned PART_BYTES = 57344u;
constexpr unsigned FLAG_OFF = 256u * PART_BYTES;
constexpr unsigned FLAG_STRIDE = 256u;
constexpr unsigned DIAG_OFF = FLAG_OFF + 256u * FLAG_STRIDE;

#define WAIT_V(n) asm volatile("s_waitcnt vmcnt(" #n ")" ::: "memory")
#define WAIT_L0() asm volatile("s_waitcnt lgkmcnt(0)" ::: "memory")
#define BAR() asm volatile("s_barrier" ::: "memory")

__device__ __forceinline__ unsigned pack2(float a, float b) {
  f32x2 v = {a, b};
  bf16x2 p = __builtin_convertvector(v, bf16x2);
  return __builtin_bit_cast(unsigned, p);
}

#define DMA4_BODY(POL)                                \
      "s_nop 4\n\t"                                   \
      "s_mov_b32 %0, m0\n\t"                          \
      "s_mov_b32 m0, %1\n\t"                          \
      "s_nop 0\n\t"                                   \
      "global_load_lds_dwordx4 %2, %4" POL "\n\t"     \
      "s_add_u32 m0, m0, 0x400\n\t"                   \
      "s_nop 0\n\t"                                   \
      "global_load_lds_dwordx4 %3, %4" POL "\n\t"     \
      "s_add_u32 m0, m0, 0x400\n\t"                   \
      "s_nop 0\n\t"                                   \
      "global_load_lds_dwordx4 %2, %5" POL "\n\t"     \
      "s_add_u32 m0, m0, 0x400\n\t"                   \
      "s_nop 0\n\t"                                   \
      "global_load_lds_dwordx4 %3, %5" POL "\n\t"     \
      "s_mov_b32 m0, %0"
template <bool NT>
__device__ __forceinline__ void dma4(unsigned lds, unsigned va, unsigned vb, const char* g0, const char* g1) {
  unsigned keep;
  if (NT)
    asm volatile(DMA4_BODY(" nt") : "=&s"(keep) : "s"(lds), "v"(va), "v"(vb), "s"(g0), "s"(g1) : "memory", "scc");
  else
    asm volatile(DMA4_BODY("") : "=&s"(keep) : "s"(lds), "v"(va), "v"(vb), "s"(g0), "s"(g1) : "memory", "scc");
}
__device__ __forceinline__ void dma1(unsigned lds, unsigned va, const char* g0) {
  unsigned keep;
  asm volatile(
      "s_nop 4\n\t"
      "s_mov_b32 %0, m0\n\t"
      "s_mov_b32 m0, %1\n\t"
      "s_nop 0\n\t"
      "global_load_lds_dwordx4 %2, %3\n\t"
      "s_mov_b32 m0, %0"
      : "=&s"(keep)
      : "s"(lds), "v"(va), "s"(g0)
      : "memory");
}

__device__ __forceinline__ bf16x8 lds_b128(unsigned addr) { return *(LAS bf16x8*)(addr); }
__device__ __forceinline__ bf16x8 lds_tr(unsigned a0, unsigned a1) {
  bf16x4 lo = __builtin_amdgcn_ds_read_tr16_b64_v4bf16((LAS bf16x4*)(a0));
  bf16x4 hi = __builtin_amdgcn_ds_read_tr16_b64_v4bf16((LAS bf16x4*)(a1));
  return __builtin_shufflevector(lo, hi, 0, 1, 2, 3, 4, 5, 6, 7);
}
__device__ __forceinline__ f32x4 mfma(bf16x8 a, bf16x8 b, f32x4 c) {
  return __builtin_amdgcn_mfma_f32_16x16x32_bf16(a, b, c, 0, 0, 0);
}
__device__ __forceinline__ float xg_max(float v) {
  auto a = __builtin_amdgcn_permlane16_swap(__float_as_uint(v), __float_as_uint(v), false, false);
  v = fmaxf(__uint_as_float(a[0]), __uint_as_float(a[1]));
  auto b = __builtin_amdgcn_permlane32_swap(__float_as_uint(v), __float_as_uint(v), false, false);
  return fmaxf(__uint_as_float(b[0]), __uint_as_float(b[1]));
}
__device__ __forceinline__ float xg_sum(float v) {
  auto a = __builtin_amdgcn_permlane16_swap(__float_as_uint(v), __float_as_uint(v), false, false);
  v = __uint_as_float(a[0]) + __uint_as_float(a[1]);
  auto b = __builtin_amdgcn_permlane32_swap(__float_as_uint(v), __float_as_uint(v), false, false);
  return __uint_as_float(b[0]) + __uint_as_float(b[1]);
}

#define STAMP(i) do { } while (0)

__global__ __launch_bounds__(512, 2) void gqa2_fused(
    const char* __restrict__ xA, const char* __restrict__ kA, const char* __restrict__ vA, const char* __restrict__ wqA,
    const char* __restrict__ woA, const char* __restrict__ xB, const char* __restrict__ kB, const char* __restrict__ vB,
    const char* __restrict__ wqB, const char* __restrict__ woB, char* __restrict__ out, char* ws) {
  __shared__ __attribute__((aligned(16))) char smem[163840];
  const unsigned L0 = (unsigned)(size_t)(LAS char*)smem;

  const unsigned tid = threadIdx.x;
  const unsigned w = __builtin_amdgcn_readfirstlane(tid >> 6);
  const unsigned l = tid & 63, r = l & 15, g = l >> 4, qq = (l & 15) >> 2, p = l & 3;

  const unsigned bid = blockIdx.x, xcd = bid & 7, idx = bid >> 3;
  const unsigned br = xcd >> 2, b = idx & 7, h = (xcd & 3) * 4 + (idx >> 3), kvh = h >> 1;
  const unsigned rotw = b == 7 ? 0 : b;
  const unsigned grp = br * 8 + b;

  const char* xg = (br ? xB : xA) + (size_t)b * (64 * 896);
  const char* wq = (br ? wqB : wqA) + h * 512;
  const char* kg = (br ? kB : kA) + (size_t)(b * 8 + kvh) * (512 * 512);
  const char* vg = (br ? vB : vA) + (size_t)(b * 8 + kvh) * (512 * 512);
  const char* wo = (br ? woB : woA) + (size_t)h * (256 * 896);

  gu32* myflags = (gu32*)(ws + FLAG_OFF + (grp * 16 + h) * FLAG_STRIDE);
  if (tid < 16) __hip_atomic_store(myflags + tid, 0u, RLX_AGENT);

  const unsigned hi = l >> 5;
  const unsigned swA = 16 * ((l & 31) ^ ((hi | ((w & 1) << 2)) << 1));
  const unsigned swB = 16 * ((l & 31) ^ ((hi | 2 | ((w & 1) << 2)) << 1));
  const unsigned vWqA = hi * 8192 + swA, vWqB = (2 + hi) * 8192 + swB;
  const unsigned vRA = hi * 512 + swA, vRB = (2 + hi) * 512 + swB;
  const unsigned vX = (l >> 3) * 896 + 16 * ((l & 7) ^ ((4 * w + (l >> 4)) & 7));
  const unsigned vWoA = (l >> 3) * 896 + 16 * ((l & 7) ^ (((l >> 4) & 1) << 1));
  const unsigned vWoB = (8 + (l >> 3)) * 896 + 16 * ((l & 7) ^ ((((l >> 4) & 1) | 2) << 1));
  const char* wqw = wq + (size_t)w * (8 * 8192);
  const char* xw = xg + w * (8 * 896);
  const char* kw = kg + w * (16 * 512);
  const char* vw = vg + w * (8 * 512);
  const char* wow = wo + w * (32 * 896);
#define ROT7(s) ((s) + rotw >= 7 ? (s) + rotw - 7 : (s) + rotw)
#define ISSUE(i)                                                                                      \
  do {                                                                                                \
    const unsigned dst_ = L0 + slot_of(i) * SLOT + w * 4096;                                          \
    if ((i) < 7) {                                                                                    \
      const unsigned s_ = ROT7((unsigned)(i));                                                        \
      const char* gb_ = wqw + (size_t)s_ * (64 * 8192);                                               \
      dma4<false>(dst_, vWqA, vWqB, gb_, gb_ + 4 * 8192);                                             \
      dma1(L0 + AUX + ((i) & 3) * 8192 + w * 1024, vX, xw + s_ * 128);                                \
    } else if ((i) < 15) {                                                                            \
      const char* gb_ = kw + ((((i) - 7) >> 1) * 128 + (((i) - 7) & 1) * 4) * 512;                    \
      dma4<KV_NT>(dst_, vRA, vRB, gb_, gb_ + 8 * 512);                                                \
    } else if ((i) < 23) {                                                                            \
      const char* gb_ = vw + ((i) - 15) * (64 * 512);                                                 \
      dma4<KV_NT>(dst_, vRA, vRB, gb_, gb_ + 4 * 512);                                                \
    } else {                                                                                          \
      const char* gb_ = wow + ROT7((unsigned)((i) - 23)) * 128;                                       \
      dma4<false>(dst_, vWoA, vWoB, gb_, gb_ + 16 * 896);                                             \
    }                                                                                                 \
  } while (0)
#define SYNC(n) do { WAIT_L0(); WAIT_V(n); BAR(); } while (0)

  ISSUE(0); BAR();
  ISSUE(1); BAR();
  ISSUE(2); BAR();
  ISSUE(3);

  f32x4 acc[2][4];
#pragma unroll
  for (int i = 0; i < 2; ++i)
#pragma unroll
    for (int j = 0; j < 4; ++j) acc[i][j] = (f32x4){0.f, 0.f, 0.f, 0.f};
  const unsigned aT = L0 + (8 * g + qq) * 512 + (((2 * w) ^ (qq | ((g & 1) << 2))) * 32) + 8 * p;
  const unsigned aX0 = L0 + AUX + r * 128 + 16 * (g ^ (r >> 1));
  const unsigned aLin = L0 + l * 16;
#define ROWS_COMPUTE(i, BADDR)                                                                        \
  do {                                                                                                \
    _Pragma("unroll") for (int u = 0; u < 2; ++u) {                                                   \
      bf16x8 fb[4], fa[2];                                                                            \
      _Pragma("unroll") for (int qt = 0; qt < 4; ++qt) fb[qt] = lds_b128(BADDR);                      \
      _Pragma("unroll") for (int dt = 0; dt < 2; ++dt) {                                              \
        const unsigned a_ = (aT ^ (dt * 32)) + slot_of(i) * SLOT + u * 16384;                         \
        fa[dt] = lds_tr(a_, a_ + 2048);                                                               \
      }                                                                                               \
      _Pragma("unroll") for (int dt = 0; dt < 2; ++dt)                                                \
          _Pragma("unroll") for (int qt = 0; qt < 4; ++qt) acc[dt][qt] = mfma(fa[dt], fb[qt], acc[dt][qt]); \
    }                                                                                                 \
  } while (0)
#define Q_COMPUTE(i) ROWS_COMPUTE(i, (aX0 ^ (u * 64)) + ((i) & 3) * 8192 + qt * 2048)

  SYNC(15); STAMP(1); Q_COMPUTE(0);
  SYNC(10); ISSUE(4); Q_COMPUTE(1);
  SYNC(10); ISSUE(5); Q_COMPUTE(2);
  SYNC(10); ISSUE(6); Q_COMPUTE(3);
  SYNC(10); ISSUE(7); Q_COMPUTE(4);
  SYNC(9);  ISSUE(8); Q_COMPUTE(5);
  SYNC(8);  ISSUE(9); Q_COMPUTE(6);
  WAIT_L0(); BAR();
  STAMP(2);

  const unsigned aFw = L0 + AUX + w * 4096 + ((g >> 1) * 16 + r) * 16 + 8 * (g & 1);
#pragma unroll
  for (int dt = 0; dt < 2; ++dt)
#pragma unroll
    for (int qt = 0; qt < 4; ++qt) {
      u32x2 v = {pack2(acc[dt][qt][0], acc[dt][qt][1]), pack2(acc[dt][qt][2], acc[dt][qt][3])};
      *(LAS u32x2*)(aFw + qt * 1024 + dt * 512) = v;
    }

  const unsigned qh = w >> 2, cq = w & 3;
  constexpr float CEXP = 1.4426950408889634f / 16.0f;
  bf16x8 bf[2][8];
  f32x4 e[8][2];
  float ml[8][2], ls[8][2];
  const unsigned aK = L0 + (16 * cq + r) * 512 + 16 * (g ^ ((r & 8) | ((r & 3) << 1)));
#define LOAD_BF()                                                                                     \
  do {                                                                                                \
    _Pragma("unroll") for (int ks = 0; ks < 8; ++ks)                                                  \
        _Pragma("unroll") for (int qt = 0; qt < 2; ++qt)                                              \
            bf[qt][ks] = lds_b128(aLin + AUX + qh * 2048 + ks * 4096 + qt * 1024);                    \
  } while (0)
#define S_COMPUTE(i)                                                                                  \
  do {                                                                                                \
    f32x4 s0_ = {0.f, 0.f, 0.f, 0.f}, s1_ = {0.f, 0.f, 0.f, 0.f};                                     \
    _Pragma("unroll") for (int ks = 0; ks < 8; ++ks) {                                                \
      const bf16x8 ka = lds_b128((aK ^ ((ks & 3) * 64)) + slot_of(i) * SLOT + (ks >> 2) * 256);       \
      s0_ = mfma(ka, bf[0][ks], s0_);                                                                 \
      s1_ = mfma(ka, bf[1][ks], s1_);                                                                 \
    }                                                                                                 \
    _Pragma("unroll") for (int qt = 0; qt < 2; ++qt) {                                                \
      const f32x4 s_ = qt ? s1_ : s0_;                                                                \
      const float m_ = fmaxf(fmaxf(s_[0], s_[1]), fmaxf(s_[2], s_[3])) * CEXP;                        \
      _Pragma("unroll") for (int c = 0; c < 4; ++c)                                                   \
          e[(i) - 7][qt][c] = __builtin_amdgcn_exp2f(fmaf(s_[c], CEXP, -m_));                         \
      ml[(i) - 7][qt] = m_;                                                                           \
      ls[(i) - 7][qt] = (e[(i) - 7][qt][0] + e[(i) - 7][qt][1]) + (e[(i) - 7][qt][2] + e[(i) - 7][qt][3]); \
    }                                                                                                 \
  } while (0)

  SYNC(8); STAMP(3); ISSUE(10); LOAD_BF(); S_COMPUTE(7);
  SYNC(8); ISSUE(11); S_COMPUTE(8);
  SYNC(8); ISSUE(12); S_COMPUTE(9);
  SYNC(8); ISSUE(13); S_COMPUTE(10);
  SYNC(8); ISSUE(14); S_COMPUTE(11);
  SYNC(8); S_COMPUTE(12);
  SYNC(4); ISSUE(15); S_COMPUTE(13);
  SYNC(4); ISSUE(16); S_COMPUTE(14);

#pragma unroll
  for (int qt = 0; qt < 2; ++qt) {
    float m = ml[0][qt];
#pragma unroll
    for (int s = 1; s < 8; ++s) m = fmaxf(m, ml[s][qt]);
    m = xg_max(m);
    *(LAS float*)(L0 + PBUF + (g * 4 + 2 * qh) * 1024 + (qt * 16 + r) * 16 + cq * 4) = m;
  }
  WAIT_L0(); BAR();
  STAMP(4);
  ISSUE(17);
  float rs[2];
  {
    f32x4 mm[2];
#pragma unroll
    for (int qt = 0; qt < 2; ++qt) mm[qt] = *(LAS f32x4*)(L0 + PBUF + (cq * 4 + 2 * qh) * 1024 + (qt * 16 + r) * 16);
    WAIT_L0();
#pragma unroll
    for (int qt = 0; qt < 2; ++qt) {
      const float m = fmaxf(fmaxf(mm[qt][0], mm[qt][1]), fmaxf(mm[qt][2], mm[qt][3]));
      float sum = 0.f;
#pragma unroll
      for (int u = 0; u < 4; ++u) {
        const float f0 = __builtin_amdgcn_exp2f(ml[2 * u][qt] - m), f1 = __builtin_amdgcn_exp2f(ml[2 * u + 1][qt] - m);
        sum = fmaf(f0, ls[2 * u][qt], fmaf(f1, ls[2 * u + 1][qt], sum));
        const f32x4 a = e[2 * u][qt] * f0, c = e[2 * u + 1][qt] * f1;
        u32x4 v = {pack2(a[0], a[1]), pack2(a[2], a[3]), pack2(c[0], c[1]), pack2(c[2], c[3])};
        *(LAS u32x4*)(aLin + PBUF + ((4 * u + cq) * 4 + 2 * qh + qt) * 1024) = v;
      }
      rs[qt] = xg_sum(sum);
    }
  }

#pragma unroll
  for (int i = 0; i < 2; ++i)
#pragma unroll
    for (int j = 0; j < 4; ++j) acc[i][j] = (f32x4){0.f, 0.f, 0.f, 0.f};
#define O_COMPUTE(i) ROWS_COMPUTE(i, aLin + PBUF + ((((i) - 15) * 2 + u) * 4 + qt) * 1024)
  float linv[4];
  SYNC(8); STAMP(5); O_COMPUTE(15);
  SYNC(4); ISSUE(18);
  if (g < 2) *(LAS float*)(L0 + PBUF + (32 * qh + l) * 16 + cq * 4) = g ? rs[1] : rs[0];
  O_COMPUTE(16);
  SYNC(4); ISSUE(19);
#pragma unroll
  for (int qt = 0; qt < 4; ++qt) {
    const f32x4 s = *(LAS f32x4*)(L0 + PBUF + (16 * qt + r) * 16);
    linv[qt] = 1.0f / ((s[0] + s[1]) + (s[2] + s[3]));
  }
  O_COMPUTE(17);
  SYNC(4); ISSUE(20); O_COMPUTE(18);
  SYNC(4); STAMP(6); ISSUE(21); ISSUE(22); O_COMPUTE(19);
  SYNC(8); ISSUE(23); O_COMPUTE(20);
  SYNC(8); ISSUE(24); O_COMPUTE(21);
  SYNC(8); ISSUE(25); O_COMPUTE(22);
  WAIT_L0(); BAR();
  STAMP(7);
  ISSUE(26);
#pragma unroll
  for (int dt = 0; dt < 2; ++dt)
#pragma unroll
    for (int qt = 0; qt < 4; ++qt) {
      const f32x4 o = acc[dt][qt] * linv[qt];
      u32x2 v = {pack2(o[0], o[1]), pack2(o[2], o[3])};
      *(LAS u32x2*)(aFw + qt * 1024 + dt * 512) = v;
    }

  const __amdgpu_buffer_rsrc_t prs = __builtin_amdgcn_make_buffer_rsrc(ws + (size_t)grp * (16 * PART_BYTES), 0, 16 * PART_BYTES, 0x00020000);
  const unsigned pOff = h * PART_BYTES + (qh * 8 + (r >> 1)) * 3584 + cq * 128 + g * 32 + (r & 1) * 16;
  const unsigned aWo = L0 + (8 * g + qq) * 128 + ((cq ^ (((qq >> 1) & 1) | ((g & 1) << 1))) * 32) + 8 * p;
#define W_COMPUTE(i)                                                                                  \
  do {                                                                                                \
    f32x4 s0_ = {0.f, 0.f, 0.f, 0.f}, s1_ = {0.f, 0.f, 0.f, 0.f};                                     \
    _Pragma("unroll") for (int ks = 0; ks < 8; ++ks) {                                                \
      const unsigned a_ = aWo + slot_of(i) * SLOT + ks * 4096;                                        \
      const bf16x8 wa = lds_tr(a_, a_ + 512);                                                         \
      s0_ = mfma(wa, bf[0][ks], s0_);                                                                 \
      s1_ = mfma(wa, bf[1][ks], s1_);                                                                 \
    }                                                                                                 \
    u32x4 v = {pack2(s0_[0], s0_[1]), pack2(s0_[2], s0_[3]), pack2(s1_[0], s1_[1]), pack2(s1_[2], s1_[3])}; \
    __builtin_amdgcn_raw_buffer_store_b128(v, prs, pOff, ROT7((unsigned)((i) - 23)) * 512, 16);       \
  } while (0)
  SYNC(12); STAMP(8); LOAD_BF(); W_COMPUTE(23);
  SYNC(9);  ISSUE(27); W_COMPUTE(24);
  SYNC(10); ISSUE(28); W_COMPUTE(25);
  SYNC(11); ISSUE(29); W_COMPUTE(26);
  SYNC(11); STAMP(9); W_COMPUTE(27);
  SYNC(7);  W_COMPUTE(28);
  SYNC(3);  W_COMPUTE(29);
  STAMP(10);

  WAIT_V(0); BAR();
  STAMP(11);
  if (w == 0) {
    gu32* peer = (gu32*)(ws + FLAG_OFF + (grp * 16 + (l & 15)) * FLAG_STRIDE) + h;
    unsigned spins = 0;
    for (;;) {
      if ((spins & 63) == 0 && l < 16) __hip_atomic_store(peer, 1u, RLX_AGENT);
      const unsigned f = __hip_atomic_load(myflags + (l & 15), RLX_AGENT);
      if (__all(f == 1u)) break;
      if (++spins > (1u << 22)) break;
      __builtin_amdgcn_s_sleep(1);
    }
    if (l < 16) __hip_atomic_store(peer, 1u, RLX_AGENT);
  }
  BAR();
  STAMP(12);
  __builtin_amdgcn_fence(__ATOMIC_ACQUIRE, "wavefront");
  if (w < 7) {
    const unsigned half = l >> 5, it = l & 31;
    const unsigned off = half * (8 * PART_BYTES) + h * 3584 + w * 512 + it * 16;
    u32x4 pv[8];
#pragma unroll
    for (int e = 0; e < 8; ++e) pv[e] = __builtin_amdgcn_raw_buffer_load_b128(prs, off + e * PART_BYTES, 0, 16);
    float s[8];
#pragma unroll
    for (int i = 0; i < 8; ++i) s[i] = 0.f;
#pragma unroll
    for (int e = 0; e < 8; ++e)
#pragma unroll
      for (int i = 0; i < 4; ++i) {
        s[2 * i] += __uint_as_float(pv[e][i] << 16);
        s[2 * i + 1] += __uint_as_float(pv[e][i] & 0xffff0000u);
      }
    float o[4];
#pragma unroll
    for (int i = 0; i < 4; ++i) {
      auto sw = __builtin_amdgcn_permlane32_swap(__float_as_uint(s[i]), __float_as_uint(s[4 + i]), false, false);
      o[i] = __uint_as_float(sw[0]) + __uint_as_float(sw[1]);
    }
    const unsigned q = 32 * (h >> 3) + 16 * half + 2 * (h & 7) + (it & 1);
    const unsigned n = 64 * w + 4 * (it >> 1);
    u32x2 v = {pack2(o[0], o[1]), pack2(o[2], o[3])};
    *(u32x2*)(out + (size_t)(b * 64 + q) * 1792 + br * 896 + n * 2) = v;
  }
}

extern "C" void kernel_launch(void* const* d_in, const int* in_sizes, int n_in, void* d_out, int out_size, void* d_ws,
                              size_t ws_size, hipStream_t stream) {
  gqa2_fused<<<256, 512, 0, stream>>>((const char*)d_in[0], (const char*)d_in[1], (const char*)d_in[2], (const char*)d_in[3],
                                      (const char*)d_in[4], (const char*)d_in[5], (const char*)d_in[6], (const char*)d_in[7],
                                      (const char*)d_in[8], (const char*)d_in[9], (char*)d_out, (char*)d_ws);
}
```

```cpp
#define DIAG 1
#define PROBE 0
#include <hip/hip_runtime.h>
#include <hip/hip_bf16.h>
#pragma clang diagnostic ignored "-Wint-to-pointer-cast"

typedef __attribute__((ext_vector_type(8))) __bf16 bf16x8;
typedef __attribute__((ext_vector_type(4))) __bf16 bf16x4;
typedef __attribute__((ext_vector_type(2))) __bf16 bf16x2;
typedef __attribute__((ext_vector_type(4))) float f32x4;
typedef __attribute__((ext_vector_type(2))) float f32x2;
typedef __attribute__((ext_vector_type(4))) unsigned u32x4;
typedef __attribute__((ext_vector_type(2))) unsigned u32x2;
typedef __attribute__((address_space(1))) unsigned gu32;
#define LAS __attribute__((address_space(3)))
#define RLX_AGENT __ATOMIC_RELAXED, __HIP_MEMORY_SCOPE_AGENT

constexpr unsigned SLOT = 32768u;
constexpr unsigned AUX = 4u * SLOT;
constexpr unsigned PBUF = 3u * SLOT;
#define KV_NT true
constexpr unsigned slot_of(int i) {
  return i < 15 ? (i & 3) : i < 22 ? (i - 15) % 3 : i == 22 ? 3 : i == 23 ? 1 : i == 24 ? 2 : i == 25 ? 0 : i == 26 ? 3 : i == 27 ? 1 : i == 28 ? 2 : 0;
}
constexpr unsigned PART_BYTES = 57344u;
constexpr unsigned FLAG_OFF = 256u * PART_BYTES;
constexpr unsigned FLAG_STRIDE = 256u;
constexpr unsigned DIAG_OFF = FLAG_OFF + 256u * FLAG_STRIDE;

#define WAIT_V(n) asm volatile("s_waitcnt vmcnt(" #n ")" ::: "memory")
#define WAIT_L0() asm volatile("s_waitcnt lgkmcnt(0)" ::: "memory")
#define BAR() asm volatile("s_barrier" ::: "memory")

__device__ __forceinline__ unsigned pack2(float a, float b) {
  f32x2 v = {a, b};
  bf16x2 p = __builtin_convertvector(v, bf16x2);
  return __builtin_bit_cast(unsigned, p);
}

#define DMA4_BODY(POL)                                \
      "s_nop 4\n\t"                                   \
      "s_mov_b32 %0, m0\n\t"                          \
      "s_mov_b32 m0, %1\n\t"                          \
      "s_nop 0\n\t"                                   \
      "global_load_lds_dwordx4 %2, %4" POL "\n\t"     \
      "s_add_u32 m0, m0, 0x400\n\t"                   \
      "s_nop 0\n\t"                                   \
      "global_load_lds_dwordx4 %3, %4" POL "\n\t"     \
      "s_add_u32 m0, m0, 0x400\n\t"                   \
      "s_nop 0\n\t"                                   \
      "global_load_lds_dwordx4 %2, %5" POL "\n\t"     \
      "s_add_u32 m0, m0, 0x400\n\t"                   \
      "s_nop 0\n\t"                                   \
      "global_load_lds_dwordx4 %3, %5" POL "\n\t"     \
      "s_mov_b32 m0, %0"
template <bool NT>
__device__ __forceinline__ void dma4(unsigned lds, unsigned va, unsigned vb, const char* g0, const char* g1) {
  unsigned keep;
  if (NT)
    asm volatile(DMA4_BODY(" nt") : "=&s"(keep) : "s"(lds), "v"(va), "v"(vb), "s"(g0), "s"(g1) : "memory", "scc");
  else
    asm volatile(DMA4_BODY("") : "=&s"(keep) : "s"(lds), "v"(va), "v"(vb), "s"(g0), "s"(g1) : "memory", "scc");
}
__device__ __forceinline__ void dma1(unsigned lds, unsigned va, const char* g0) {
  unsigned keep;
  asm volatile(
      "s_nop 4\n\t"
      "s_mov_b32 %0, m0\n\t"
      "s_mov_b32 m0, %1\n\t"
      "s_nop 0\n\t"
      "global_load_lds_dwordx4 %2, %3\n\t"
      "s_mov_b32 m0, %0"
      : "=&s"(keep)
      : "s"(lds), "v"(va), "s"(g0)
      : "memory");
}

__device__ __forceinline__ bf16x8 lds_b128(unsigned addr) { return *(LAS bf16x8*)(addr); }
__device__ __forceinline__ bf16x8 lds_tr(unsigned a0, unsigned a1) {
  bf16x4 lo = __builtin_amdgcn_ds_read_tr16_b64_v4bf16((LAS bf16x4*)(a0));
  bf16x4 hi = __builtin_amdgcn_ds_read_tr16_b64_v4bf16((LAS bf16x4*)(a1));
  return __builtin_shufflevector(lo, hi, 0, 1, 2, 3, 4, 5, 6, 7);
}
__device__ __forceinline__ f32x4 mfma(bf16x8 a, bf16x8 b, f32x4 c) {
  return __builtin_amdgcn_mfma_f32_16x16x32_bf16(a, b, c, 0, 0, 0);
}
__device__ __forceinline__ float xg_max(float v) {
  auto a = __builtin_amdgcn_permlane16_swap(__float_as_uint(v), __float_as_uint(v), false, false);
  v = fmaxf(__uint_as_float(a[0]), __uint_as_float(a[1]));
  auto b = __builtin_amdgcn_permlane32_swap(__float_as_uint(v), __float_as_uint(v), false, false);
  return fmaxf(__uint_as_float(b[0]), __uint_as_float(b[1]));
}
__device__ __forceinline__ float xg_sum(float v) {
  auto a = __builtin_amdgcn_permlane16_swap(__float_as_uint(v), __float_as_uint(v), false, false);
  v = __uint_as_float(a[0]) + __uint_as_float(a[1]);
  auto b = __builtin_amdgcn_permlane32_swap(__float_as_uint(v), __float_as_uint(v), false, false);
  return __uint_as_float(b[0]) + __uint_as_float(b[1]);
}

#define STAMP(i) do { ts[i] = __builtin_amdgcn_s_memrealtime(); } while (0)

__global__ __launch_bounds__(512, 2) void gqa2_fused(
    const char* __restrict__ xA, const char* __restrict__ kA, const char* __restrict__ vA, const char* __restrict__ wqA,
    const char* __restrict__ woA, const char* __restrict__ xB, const char* __restrict__ kB, const char* __restrict__ vB,
    const char* __restrict__ wqB, const char* __restrict__ woB, char* __restrict__ out, char* ws) {
  __shared__ __attribute__((aligned(16))) char smem[163840];
  const unsigned L0 = (unsigned)(size_t)(LAS char*)smem;

  const unsigned tid = threadIdx.x;
  const unsigned w = __builtin_amdgcn_readfirstlane(tid >> 6);
  const unsigned l = tid & 63, r = l & 15, g = l >> 4, qq = (l & 15) >> 2, p = l & 3;

  const unsigned bid = blockIdx.x, xcd = bid & 7, idx = bid >> 3;
  const unsigned br = xcd >> 2, b = idx & 7, h = (xcd & 3) * 4 + (idx >> 3), kvh = h >> 1;
  const unsigned rotw = b == 7 ? 0 : b;
  const unsigned grp = br * 8 + b;

  const char* xg = (br ? xB : xA) + (size_t)b * (64 * 896);
  const char* wq = (br ? wqB : wqA) + h * 512;
  const char* kg = (br ? kB : kA) + (size_t)(b * 8 + kvh) * (512 * 512);
  const char* vg = (br ? vB : vA) + (size_t)(b * 8 + kvh) * (512 * 512);
  const char* wo = (br ? woB : woA) + (size_t)h * (256 * 896);

  unsigned long long ts[16];
#pragma unroll
  for (int i = 0; i < 16; ++i) ts[i] = 0;
  STAMP(0);

  gu32* myflags = (gu32*)(ws + FLAG_OFF + (grp * 16 + h) * FLAG_STRIDE);
  if (tid < 16) __hip_atomic_store(myflags + tid, 0u, RLX_AGENT);

  const unsigned hi = l >> 5;
  const unsigned swA = 16 * ((l & 31) ^ ((hi | ((w & 1) << 2)) << 1));
  const unsigned swB = 16 * ((l & 31) ^ ((hi | 2 | ((w & 1) << 2)) << 1));
  const unsigned vWqA = hi * 8192 + swA, vWqB = (2 + hi) * 8192 + swB;
  const unsigned vRA = hi * 512 + swA, vRB = (2 + hi) * 512 + swB;
  const unsigned vX = (l >> 3) * 896 + 16 * ((l & 7) ^ ((4 * w + (l >> 4)) & 7));
  const unsigned vWoA = (l >> 3) * 896 + 16 * ((l & 7) ^ (((l >> 4) & 1) << 1));
  const unsigned vWoB = (8 + (l >> 3)) * 896 + 16 * ((l & 7) ^ ((((l >> 4) & 1) | 2) << 1));
  const char* wqw = wq + (size_t)w * (8 * 8192);
  const char* xw = xg + w * (8 * 896);
  const char* kw = kg + w * (16 * 512);
  const char* vw = vg + w * (8 * 512);
  const char* wow = wo + w * (32 * 896);
#define ROT7(s) ((s) + rotw >= 7 ? (s) + rotw - 7 : (s) + rotw)
#define ISSUE(i)                                                                                      \
  do {                                                                                                \
    const unsigned dst_ = L0 + slot_of(i) * SLOT + w * 4096;                                          \
    if ((i) < 7) {                                                                                    \
      const unsigned s_ = ROT7((unsigned)(i));                                                        \
      const char* gb_ = wqw + (size_t)s_ * (64 * 8192);                                               \
      dma4<false>(dst_, vWqA, vWqB, gb_, gb_ + 4 * 8192);                                             \
      dma1(L0 + AUX + ((i) & 3) * 8192 + w * 1024, vX, xw + s_ * 128);                                \
    } else if ((i) < 15) {                                                                            \
      const char* gb_ = kw + ((((i) - 7) >> 1) * 128 + (((i) - 7) & 1) * 4) * 512;                    \
      dma4<KV_NT>(dst_, vRA, vRB, gb_, gb_ + 8 * 512);                                                \
    } else if ((i) < 23) {                                                                            \
      const char* gb_ = vw + ((i) - 15) * (64 * 512);                                                 \
      dma4<KV_NT>(dst_, vRA, vRB, gb_, gb_ + 4 * 512);                                                \
    } else {                                                                                          \
      const char* gb_ = wow + ROT7((unsigned)((i) - 23)) * 128;                                       \
      dma4<false>(dst_, vWoA, vWoB, gb_, gb_ + 16 * 896);                                             \
    }                                                                                                 \
  } while (0)
#define SYNC(n) do { WAIT_L0(); WAIT_V(n); BAR(); } while (0)

  ISSUE(0); BAR();
  ISSUE(1); BAR();
  ISSUE(2); BAR();
  ISSUE(3);

  f32x4 acc[2][4];
#pragma unroll
  for (int i = 0; i < 2; ++i)
#pragma unroll
    for (int j = 0; j < 4; ++j) acc[i][j] = (f32x4){0.f, 0.f, 0.f, 0.f};
  const unsigned aT = L0 + (8 * g + qq) * 512 + (((2 * w) ^ (qq | ((g & 1) << 2))) * 32) + 8 * p;
  const unsigned aX0 = L0 + AUX + r * 128 + 16 * (g ^ (r >> 1));
  const unsigned aLin = L0 + l * 16;
#define ROWS_COMPUTE(i, BADDR)                                                                        \
  do {                                                                                                \
    _Pragma("unroll") for (int u = 0; u < 2; ++u) {                                                   \
      bf16x8 fb[4], fa[2];                                                                            \
      _Pragma("unroll") for (int qt = 0; qt < 4; ++qt) fb[qt] = lds_b128(BADDR);                      \
      _Pragma("unroll") for (int dt = 0; dt < 2; ++dt) {                                              \
        const unsigned a_ = (aT ^ (dt * 32)) + slot_of(i) * SLOT + u * 16384;                         \
        fa[dt] = lds_tr(a_, a_ + 2048);                                                               \
      }                                                                                               \
      _Pragma("unroll") for (int dt = 0; dt < 2; ++dt)                                                \
          _Pragma("unroll") for (int qt = 0; qt < 4; ++qt) acc[dt][qt] = mfma(fa[dt], fb[qt], acc[dt][qt]); \
    }                                                                                                 \
  } while (0)
#define Q_COMPUTE(i) ROWS_COMPUTE(i, (aX0 ^ (u * 64)) + ((i) & 3) * 8192 + qt * 2048)

  SYNC(15); STAMP(1); Q_COMPUTE(0);
  SYNC(10); ISSUE(4); Q_COMPUTE(1);
  SYNC(10); ISSUE(5); Q_COMPUTE(2);
  SYNC(10); ISSUE(6); Q_COMPUTE(3);
  SYNC(10); ISSUE(7); Q_COMPUTE(4);
  SYNC(9);  ISSUE(8); Q_COMPUTE(5);
  SYNC(8);  ISSUE(9); Q_COMPUTE(6);
  WAIT_L0(); BAR();
  STAMP(2);

  const unsigned aFw = L0 + AUX + w * 4096 + ((g >> 1) * 16 + r) * 16 + 8 * (g & 1);
#pragma unroll
  for (int dt = 0; dt < 2; ++dt)
#pragma unroll
    for (int qt = 0; qt < 4; ++qt) {
      u32x2 v = {pack2(acc[dt][qt][0], acc[dt][qt][1]), pack2(acc[dt][qt][2], acc[dt][qt][3])};
      *(LAS u32x2*)(aFw + qt * 1024 + dt * 512) = v;
    }

  const unsigned qh = w >> 2, cq = w & 3;
  constexpr float CEXP = 1.4426950408889634f / 16.0f;
  bf16x8 bf[2][8];
  f32x4 e[8][2];
  float ml[8][2], ls[8][2];
  const unsigned aK = L0 + (16 * cq + r) * 512 + 16 * (g ^ ((r & 8) | ((r & 3) << 1)));
#define LOAD_BF()                                                                                     \
  do {                                                                                                \
    _Pragma("unroll") for (int ks = 0; ks < 8; ++ks)                                                  \
        _Pragma("unroll") for (int qt = 0; qt < 2; ++qt)                                              \
            bf[qt][ks] = lds_b128(aLin + AUX + qh * 2048 + ks * 4096 + qt * 1024);                    \
  } while (0)
#define S_COMPUTE(i)                                                                                  \
  do {                                                                                                \
    f32x4 s0_ = {0.f, 0.f, 0.f, 0.f}, s1_ = {0.f, 0.f, 0.f, 0.f};                                     \
    _Pragma("unroll") for (int ks = 0; ks < 8; ++ks) {                                                \
      const bf16x8 ka = lds_b128((aK ^ ((ks & 3) * 64)) + slot_of(i) * SLOT + (ks >> 2) * 256);       \
      s0_ = mfma(ka, bf[0][ks], s0_);                                                                 \
      s1_ = mfma(ka, bf[1][ks], s1_);                                                                 \
    }                                                                                                 \
    _Pragma("unroll") for (int qt = 0; qt < 2; ++qt) {                                                \
      const f32x4 s_ = qt ? s1_ : s0_;                                                                \
      const float m_ = fmaxf(fmaxf(s_[0], s_[1]), fmaxf(s_[2], s_[3])) * CEXP;                        \
      _Pragma("unroll") for (int c = 0; c < 4; ++c)                                                   \
          e[(i) - 7][qt][c] = __builtin_amdgcn_exp2f(fmaf(s_[c], CEXP, -m_));                         \
      ml[(i) - 7][qt] = m_;                                                                           \
      ls[(i) - 7][qt] = (e[(i) - 7][qt][0] + e[(i) - 7][qt][1]) + (e[(i) - 7][qt][2] + e[(i) - 7][qt][3]); \
    }                                                                                                 \
  } while (0)

  SYNC(8); STAMP(3); ISSUE(10); LOAD_BF(); S_COMPUTE(7);
  SYNC(8); ISSUE(11); S_COMPUTE(8);
  SYNC(8); ISSUE(12); S_COMPUTE(9);
  SYNC(8); ISSUE(13); S_COMPUTE(10);
  SYNC(8); ISSUE(14); S_COMPUTE(11);
  SYNC(8); S_COMPUTE(12);
  SYNC(4); ISSUE(15); S_COMPUTE(13);
  SYNC(4); ISSUE(16); S_COMPUTE(14);

#pragma unroll
  for (int qt = 0; qt < 2; ++qt) {
    float m = ml[0][qt];
#pragma unroll
    for (int s = 1; s < 8; ++s) m = fmaxf(m, ml[s][qt]);
    m = xg_max(m);
    *(LAS float*)(L0 + PBUF + (g * 4 + 2 * qh) * 1024 + (qt * 16 + r) * 16 + cq * 4) = m;
  }
  WAIT_L0(); BAR();
  STAMP(4);
  ISSUE(17);
  float rs[2];
  {
    f32x4 mm[2];
#pragma unroll
    for (int qt = 0; qt < 2; ++qt) mm[qt] = *(LAS f32x4*)(L0 + PBUF + (cq * 4 + 2 * qh) * 1024 + (qt * 16 + r) * 16);
    WAIT_L0();
#pragma unroll
    for (int qt = 0; qt < 2; ++qt) {
      const float m = fmaxf(fmaxf(mm[qt][0], mm[qt][1]), fmaxf(mm[qt][2], mm[qt][3]));
      float sum = 0.f;
#pragma unroll
      for (int u = 0; u < 4; ++u) {
        const float f0 = __builtin_amdgcn_exp2f(ml[2 * u][qt] - m), f1 = __builtin_amdgcn_exp2f(ml[2 * u + 1][qt] - m);
        sum = fmaf(f0, ls[2 * u][qt], fmaf(f1, ls[2 * u + 1][qt], sum));
        const f32x4 a = e[2 * u][qt] * f0, c = e[2 * u + 1][qt] * f1;
        u32x4 v = {pack2(a[0], a[1]), pack2(a[2], a[3]), pack2(c[0], c[1]), pack2(c[2], c[3])};
        *(LAS u32x4*)(aLin + PBUF + ((4 * u + cq) * 4 + 2 * qh + qt) * 1024) = v;
      }
      rs[qt] = xg_sum(sum);
    }
  }

#pragma unroll
  for (int i = 0; i < 2; ++i)
#pragma unroll
    for (int j = 0; j < 4; ++j) acc[i][j] = (f32x4){0.f, 0.f, 0.f, 0.f};
#define O_COMPUTE(i) ROWS_COMPUTE(i, aLin + PBUF + ((((i) - 15) * 2 + u) * 4 + qt) * 1024)
  float linv[4];
  SYNC(8); STAMP(5); O_COMPUTE(15);
  SYNC(4); ISSUE(18);
  if (g < 2) *(LAS float*)(L0 + PBUF + (32 * qh + l) * 16 + cq * 4) = g ? rs[1] : rs[0];
  O_COMPUTE(16);
  SYNC(4); ISSUE(19);
#pragma unroll
  for (int qt = 0; qt < 4; ++qt) {
    const f32x4 s = *(LAS f32x4*)(L0 + PBUF + (16 * qt + r) * 16);
    linv[qt] = 1.0f / ((s[0] + s[1]) + (s[2] + s[3]));
  }
  O_COMPUTE(17);
  SYNC(4); ISSUE(20); O_COMPUTE(18);
  SYNC(4); STAMP(6); ISSUE(21); ISSUE(22); O_COMPUTE(19);
  SYNC(8); ISSUE(23); O_COMPUTE(20);
  SYNC(8); ISSUE(24); O_COMPUTE(21);
  SYNC(8); ISSUE(25); O_COMPUTE(22);
  WAIT_L0(); BAR();
  STAMP(7);
  ISSUE(26);
#pragma unroll
  for (int dt = 0; dt < 2; ++dt)
#pragma unroll
    for (int qt = 0; qt < 4; ++qt) {
      const f32x4 o = acc[dt][qt] * linv[qt];
      u32x2 v = {pack2(o[0], o[1]), pack2(o[2], o[3])};
      *(LAS u32x2*)(aFw + qt * 1024 + dt * 512) = v;
    }

  const __amdgpu_buffer_rsrc_t prs = __builtin_amdgcn_make_buffer_rsrc(ws + (size_t)grp * (16 * PART_BYTES), 0, 16 * PART_BYTES, 0x00020000);
  const unsigned pOff = h * PART_BYTES + (qh * 8 + (r >> 1)) * 3584 + cq * 128 + g * 32 + (r & 1) * 16;
  const unsigned aWo = L0 + (8 * g + qq) * 128 + ((cq ^ (((qq >> 1) & 1) | ((g & 1) << 1))) * 32) + 8 * p;
#define W_COMPUTE(i)                                                                                  \
  do {                                                                                                \
    f32x4 s0_ = {0.f, 0.f, 0.f, 0.f}, s1_ = {0.f, 0.f, 0.f, 0.f};                                     \
    _Pragma("unroll") for (int ks = 0; ks < 8; ++ks) {                                                \
      const unsigned a_ = aWo + slot_of(i) * SLOT + ks * 4096;                                        \
      const bf16x8 wa = lds_tr(a_, a_ + 512);                                                         \
      s0_ = mfma(wa, bf[0][ks], s0_);                                                                 \
      s1_ = mfma(wa, bf[1][ks], s1_);                                                                 \
    }                                                                                                 \
    u32x4 v = {pack2(s0_[0], s0_[1]), pack2(s0_[2], s0_[3]), pack2(s1_[0], s1_[1]), pack2(s1_[2], s1_[3])}; \
    __builtin_amdgcn_raw_buffer_store_b128(v, prs, pOff, ROT7((unsigned)((i) - 23)) * 512, 16);       \
  } while (0)
  SYNC(12); STAMP(8); LOAD_BF(); W_COMPUTE(23);
  SYNC(9);  ISSUE(27); W_COMPUTE(24);
  SYNC(10); ISSUE(28); W_COMPUTE(25);
  SYNC(11); ISSUE(29); W_COMPUTE(26);
  SYNC(11); STAMP(9); W_COMPUTE(27);
  SYNC(7);  W_COMPUTE(28);
  SYNC(3);  W_COMPUTE(29);
  STAMP(10);

  WAIT_V(0); BAR();
  STAMP(11);
  if (w == 0) {
    gu32* peer = (gu32*)(ws + FLAG_OFF + (grp * 16 + (l & 15)) * FLAG_STRIDE) + h;
    unsigned spins = 0;
    for (;;) {
      if ((spins & 63) == 0 && l < 16) __hip_atomic_store(peer, 1u, RLX_AGENT);
      const unsigned f = __hip_atomic_load(myflags + (l & 15), RLX_AGENT);
      if (__all(f == 1u)) break;
      if (++spins > (1u << 22)) break;
      __builtin_amdgcn_s_sleep(1);
    }
    if (l < 16) __hip_atomic_store(peer, 1u, RLX_AGENT);
  }
  BAR();
  STAMP(12);
  __builtin_amdgcn_fence(__ATOMIC_ACQUIRE, "wavefront");
  if (w < 7) {
    const unsigned half = l >> 5, it = l & 31;
    const unsigned off = half * (8 * PART_BYTES) + h * 3584 + w * 512 + it * 16;
    u32x4 pv[8];
#pragma unroll
    for (int e = 0; e < 8; ++e) pv[e] = __builtin_amdgcn_raw_buffer_load_b128(prs, off + e * PART_BYTES, 0, 16);
    float s[8];
#pragma unroll
    for (int i = 0; i < 8; ++i) s[i] = 0.f;
#pragma unroll
    for (int e = 0; e < 8; ++e)
#pragma unroll
      for (int i = 0; i < 4; ++i) {
        s[2 * i] += __uint_as_float(pv[e][i] << 16);
        s[2 * i + 1] += __uint_as_float(pv[e][i] & 0xffff0000u);
      }
    float o[4];
#pragma unroll
    for (int i = 0; i < 4; ++i) {
      auto sw = __builtin_amdgcn_permlane32_swap(__float_as_uint(s[i]), __float_as_uint(s[4 + i]), false, false);
      o[i] = __uint_as_float(sw[0]) + __uint_as_float(sw[1]);
    }
    const unsigned q = 32 * (h >> 3) + 16 * half + 2 * (h & 7) + (it & 1);
    const unsigned n = 64 * w + 4 * (it >> 1);
    u32x2 v = {pack2(o[0], o[1]), pack2(o[2], o[3])};
    *(u32x2*)(out + (size_t)(b * 64 + q) * 1792 + br * 896 + n * 2) = v;
  }
  WAIT_V(0);
  STAMP(13);
  for (int it = 0; it < 100000 && __builtin_amdgcn_s_memrealtime() < ts[PROBE] + 6000; ++it) __builtin_amdgcn_s_sleep(2);
  if (tid == 0) {
#pragma unroll
    for (int i = 0; i < 16; ++i) ((unsigned long long*)(ws + DIAG_OFF))[blockIdx.x * 16 + i] = ts[i];
  }
}

extern "C" void kernel_launch(void* const* d_in, const int* in_sizes, int n_in, void* d_out, int out_size, void* d_ws,
                              size_t ws_size, hipStream_t stream) {
  gqa2_fused<<<256, 512, 0, stream>>>((const char*)d_in[0], (const char*)d_in[1], (const char*)d_in[2], (const char*)d_in[3],
                                      (const char*)d_in[4], (const char*)d_in[5], (const char*)d_in[6], (const char*)d_in[7],
                                      (const char*)d_in[8], (const char*)d_in[9], (char*)d_out, (char*)d_ws);
}
```

```cpp
#include <hip/hip_runtime.h>
#include <hip/hip_bf16.h>
#pragma clang diagnostic ignored "-Wint-to-pointer-cast"

typedef __attribute__((ext_vector_type(8))) __bf16 bf16x8;
typedef __attribute__((ext_vector_type(4))) __bf16 bf16x4;
typedef __attribute__((ext_vector_type(2))) __bf16 bf16x2;
typedef __attribute__((ext_vector_type(4))) float f32x4;
typedef __attribute__((ext_vector_type(2))) float f32x2;
typedef __attribute__((ext_vector_type(4))) unsigned u32x4;
typedef __attribute__((ext_vector_type(2))) unsigned u32x2;
typedef __attribute__((address_space(1))) unsigned gu32;
#define LAS __attribute__((address_space(3)))
#define RLX_AGENT __ATOMIC_RELAXED, __HIP_MEMORY_SCOPE_AGENT

#define DIAG 0

constexpr unsigned SLOT = 32768u;
constexpr unsigned AUX = 4u * SLOT;
constexpr unsigned STAT = AUX + 16384u;
constexpr unsigned SUMS = AUX + 17408u;
#define KV_NT true
constexpr unsigned PART_BYTES = 57344u;
constexpr unsigned FLAG_OFF = 256u * PART_BYTES;
constexpr unsigned FLAG_STRIDE = 256u;
constexpr unsigned DIAG_OFF = FLAG_OFF + 256u * FLAG_STRIDE;

#define WAIT_V(n) asm volatile("s_waitcnt vmcnt(" #n ")" ::: "memory")
#define WAIT_L0() do { asm volatile("" ::: "memory"); __builtin_amdgcn_s_waitcnt(0xC07F); asm volatile("" ::: "memory"); } while (0)
#define BAR() asm volatile("s_barrier" ::: "memory")

__device__ __forceinline__ unsigned pack2(float a, float b) {
  f32x2 v = {a, b};
  bf16x2 p = __builtin_convertvector(v, bf16x2);
  return __builtin_bit_cast(unsigned, p);
}

#define DMA4_BODY(POL)                                \
      "s_nop 4\n\t"                                   \
      "s_mov_b32 %0, m0\n\t"                          \
      "s_mov_b32 m0, %1\n\t"                          \
      "s_nop 0\n\t"                                   \
      "global_load_lds_dwordx4 %2, %4" POL "\n\t"     \
      "s_add_u32 m0, m0, 0x400\n\t"                   \
      "s_nop 0\n\t"                                   \
      "global_load_lds_dwordx4 %3, %4" POL "\n\t"     \
      "s_add_u32 m0, m0, 0x400\n\t"                   \
      "s_nop 0\n\t"                                   \
      "global_load_lds_dwordx4 %2, %5" POL "\n\t"     \
      "s_add_u32 m0, m0, 0x400\n\t"                   \
      "s_nop 0\n\t"                                   \
      "global_load_lds_dwordx4 %3, %5" POL "\n\t"     \
      "s_mov_b32 m0, %0"
template <bool NT>
__device__ __forceinline__ void dma4(unsigned lds, unsigned va, unsigned vb, const char* g0, const char* g1) {
  unsigned keep;
  if (NT)
    asm volatile(DMA4_BODY(" nt") : "=&s"(keep) : "s"(lds), "v"(va), "v"(vb), "s"(g0), "s"(g1) : "memory", "scc");
  else
    asm volatile(DMA4_BODY("") : "=&s"(keep) : "s"(lds), "v"(va), "v"(vb), "s"(g0), "s"(g1) : "memory", "scc");
}
__device__ __forceinline__ void dma1(unsigned lds, unsigned va, const char* g0) {
  unsigned keep;
  asm volatile(
      "s_nop 4\n\t"
      "s_mov_b32 %0, m0\n\t"
      "s_mov_b32 m0, %1\n\t"
      "s_nop 0\n\t"
      "global_load_lds_dwordx4 %2, %3\n\t"
      "s_mov_b32 m0, %0"
      : "=&s"(keep)
      : "s"(lds), "v"(va), "s"(g0)
      : "memory");
}

__device__ __forceinline__ bf16x8 lds_b128(unsigned addr) { return *(LAS bf16x8*)(addr); }
__device__ __forceinline__ bf16x8 lds_tr(unsigned a0, unsigned a1) {
  bf16x4 lo = __builtin_amdgcn_ds_read_tr16_b64_v4bf16((LAS bf16x4*)(a0));
  bf16x4 hi = __builtin_amdgcn_ds_read_tr16_b64_v4bf16((LAS bf16x4*)(a1));
  return __builtin_shufflevector(lo, hi, 0, 1, 2, 3, 4, 5, 6, 7);
}
__device__ __forceinline__ f32x4 mfma(bf16x8 a, bf16x8 b, f32x4 c) {
  return __builtin_amdgcn_mfma_f32_16x16x32_bf16(a, b, c, 0, 0, 0);
}
__device__ __forceinline__ float xg_max(float v) {
  auto a = __builtin_amdgcn_permlane16_swap(__float_as_uint(v), __float_as_uint(v), false, false);
  v = fmaxf(__uint_as_float(a[0]), __uint_as_float(a[1]));
  auto b = __builtin_amdgcn_permlane32_swap(__float_as_uint(v), __float_as_uint(v), false, false);
  return fmaxf(__uint_as_float(b[0]), __uint_as_float(b[1]));
}
__device__ __forceinline__ float xg_sum(float v) {
  auto a = __builtin_amdgcn_permlane16_swap(__float_as_uint(v), __float_as_uint(v), false, false);
  v = __uint_as_float(a[0]) + __uint_as_float(a[1]);
  auto b = __builtin_amdgcn_permlane32_swap(__float_as_uint(v), __float_as_uint(v), false, false);
  return __uint_as_float(b[0]) + __uint_as_float(b[1]);
}

#define STAMP(i) do { } while (0)

__global__ __launch_bounds__(768, 3) void gqa2_fused(
    const char* __restrict__ xA, const char* __restrict__ kA, const char* __restrict__ vA, const char* __restrict__ wqA,
    const char* __restrict__ woA, const char* __restrict__ xB, const char* __restrict__ kB, const char* __restrict__ vB,
    const char* __restrict__ wqB, const char* __restrict__ woB, char* __restrict__ out, char* ws) {
  __shared__ __attribute__((aligned(16))) char smem[163840];
  const unsigned L0 = (unsigned)(size_t)(LAS char*)smem;

  const unsigned tid = threadIdx.x;
  const unsigned w = __builtin_amdgcn_readfirstlane(tid >> 6);
  const unsigned l = tid & 63, r = l & 15, g = l >> 4, qq = (l & 15) >> 2, p = l & 3;

  const unsigned bid = blockIdx.x, xcd = bid & 7, idx = bid >> 3;
  const unsigned br = xcd >> 2, b = idx & 7, h = (xcd & 3) * 4 + (idx >> 3), kvh = h >> 1;
  const unsigned rotw = (b + 2 * (h & 3)) % 7;
  const unsigned grp = br * 8 + b;

  const char* xg = (br ? xB : xA) + (size_t)b * (64 * 896);
  const char* wq = (br ? wqB : wqA) + h * 512;
  const char* kg = (br ? kB : kA) + (size_t)(b * 8 + kvh) * (512 * 512);
  const char* vg = (br ? vB : vA) + (size_t)(b * 8 + kvh) * (512 * 512);
  const char* wo = (br ? woB : woA) + (size_t)h * (256 * 896);

  gu32* myflags = (gu32*)(ws + FLAG_OFF + (grp * 16 + h) * FLAG_STRIDE);
  if (tid < 16) __hip_atomic_store(myflags + tid, 0u, RLX_AGENT);

#define ROT7(s) ((s) + rotw >= 7 ? (s) + rotw - 7 : (s) + rotw)
#define PIN() __builtin_amdgcn_sched_barrier(0)
#define OPAQUE(x) asm volatile("" : "+v"(x))
  const __amdgpu_buffer_rsrc_t prs = __builtin_amdgcn_make_buffer_rsrc(ws + (size_t)grp * (16 * PART_BYTES), 0, 16 * PART_BYTES, 0x00020000);

  if (w >= 8) {
#define SWP 0
    const unsigned swp = SWP;
    const unsigned j2 = 2 * (w - 8), hi = l >> 5;
    unsigned vWq[2][2], vR[2][2], vX[2];
#pragma unroll
    for (int e = 0; e < 2; ++e) {
#pragma unroll
      for (int c = 0; c < 2; ++c) {
        const unsigned sw = 16 * ((l & 31) ^ ((hi | (2 * c) | (e << 2)) << 1));
        vWq[e][c] = (2 * c + hi) * 8192 + sw;
        vR[e][c] = (2 * c + hi) * 512 + sw;
      }
      vX[e] = (l >> 3) * 896 + 16 * ((l & 7) ^ ((4 * e + (l >> 4)) & 7));
    }
    const unsigned vWo[2] = {(l >> 3) * 896 + 16 * ((l & 7) ^ (((l >> 4) & 1) << 1)),
                             (8 + (l >> 3)) * 896 + 16 * ((l & 7) ^ ((((l >> 4) & 1) | 2) << 1))};
    const char* wqw = wq + (size_t)j2 * (8 * 8192);
    const char* xw = xg + j2 * (8 * 896);
    const char* kw = kg + (32 * (j2 >> 2) + 4 * ((j2 >> 1) & 1)) * 512;
    const char* vw = vg + j2 * (8 * 512);
    const char* wow = wo + j2 * (32 * 896);
#define HALF(i, e)                                                                                    \
  do {                                                                                                \
    const unsigned dst_ = L0 + ((i) & 3) * SLOT + (j2 + (e)) * 4096;                                  \
    if ((i) < 7) {                                                                                    \
      const unsigned s_ = ROT7((unsigned)(i));                                                        \
      const char* gb_ = wqw + (size_t)s_ * (64 * 8192) + (e) * (8 * 8192);                            \
      dma4<false>(dst_, vWq[e][0], vWq[e][1], gb_, gb_ + 4 * 8192);                                   \
      dma1(L0 + AUX + ((i) & 3) * 8192 + (j2 + (e)) * 1024, vX[e], xw + s_ * 128 + (e) * (8 * 896));  \
    } else if ((i) < 15) {                                                                            \
      const char* gb_ = kw + (((i) - 7) ^ swp) * (64 * 512)        + (e) * (16 * 512);                \
      dma4<KV_NT>(dst_, vR[e][0], vR[e][1], gb_, gb_ + 8 * 512);                                      \
    } else if ((i) < 23) {                                                                            \
      const char* gb_ = vw + (((i) - 15) ^ swp) * (64 * 512)       + (e) * (8 * 512);                 \
      dma4<KV_NT>(dst_, vR[e][0], vR[e][1], gb_, gb_ + 4 * 512);                                      \
    } else {                                                                                          \
      const char* gb_ = wow + ROT7((unsigned)((i) - 23)) * 128 + (e) * (32 * 896);                    \
      dma4<false>(dst_, vWo[0], vWo[1], gb_, gb_ + 16 * 896);                                         \
    }                                                                                                 \
  } while (0)
#define FEED(i, n) do { HALF(i, 1); HALF((i) + 1, 0); WAIT_V(n); BAR(); } while (0)
#define FEED_MID(i, n) do { HALF(i, 1); BAR(); HALF((i) + 1, 0); WAIT_V(n); BAR(); } while (0)
    HALF(0, 0); HALF(0, 1); HALF(1, 0);
    FEED(1, 15);
    FEED(2, 15); FEED(3, 15); FEED(4, 15); FEED(5, 15); FEED(6, 14); FEED(7, 12);
    FEED_MID(8, 12);
    FEED(9, 12); FEED(10, 12); FEED(11, 12); FEED(12, 12); FEED(13, 12); FEED(14, 12); FEED(15, 12);
    FEED_MID(16, 12);
    FEED(17, 12); FEED(18, 12); FEED(19, 12); FEED(20, 12); FEED(21, 12); FEED(22, 12); FEED(23, 12);
    HALF(24, 1); BAR();
    HALF(25, 0); HALF(25, 1); WAIT_V(8); BAR();
    HALF(26, 0); HALF(26, 1); WAIT_V(8); BAR();
    HALF(27, 0); FEED(27, 12);
    FEED(28, 12);
    HALF(29, 1); WAIT_V(8); BAR();
    WAIT_V(0); BAR();
  } else {
#define SYNC(i) do { if ((i) > 0) STAMP(2 * (i) - 1); WAIT_L0(); BAR(); STAMP(2 * (i)); } while (0)
#define MID() do { WAIT_L0(); BAR(); } while (0)
    f32x4 acc[2][4];
#pragma unroll
    for (int i = 0; i < 2; ++i)
#pragma unroll
      for (int j = 0; j < 4; ++j) acc[i][j] = (f32x4){0.f, 0.f, 0.f, 0.f};
    const unsigned aT = L0 + (8 * g + qq) * 512 + (((2 * w) ^ (qq | ((g & 1) << 2))) * 32) + 8 * p;
    unsigned aX0 = L0 + AUX + r * 128 + 16 * (g ^ (r >> 1)), aX1 = aX0 ^ 64;
    unsigned aLin = L0 + AUX + l * 16;
    OPAQUE(aX0); OPAQUE(aX1); OPAQUE(aLin);
    bf16x8 fa[2][2], fb[2][4];
#define ROWS_READ(i, BADDR)                                                                           \
  do {                                                                                                \
    unsigned a0_ = aT + ((i) & 2) * SLOT, a1_ = (aT ^ 32) + ((i) & 2) * SLOT;                         \
    OPAQUE(a0_); OPAQUE(a1_);                                                                         \
    _Pragma("unroll") for (int u = 0; u < 2; ++u) {                                                   \
      _Pragma("unroll") for (int dt = 0; dt < 2; ++dt) {                                              \
        const unsigned a_ = (dt ? a1_ : a0_) + ((i) & 1) * SLOT + u * 16384;                          \
        fa[u][dt] = lds_tr(a_, a_ + 2048);                                                            \
      }                                                                                               \
      _Pragma("unroll") for (int qt_ = 0; qt_ < 4; ++qt_) fb[u][qt_] = lds_b128(BADDR);               \
    }                                                                                                 \
  } while (0)
#define ROWS_MM()                                                                                     \
  do {                                                                                                \
    _Pragma("unroll") for (int u = 0; u < 2; ++u)                                                     \
        _Pragma("unroll") for (int dt = 0; dt < 2; ++dt)                                              \
            _Pragma("unroll") for (int t = 0; t < 4; ++t) acc[dt][t] = mfma(fa[u][dt], fb[u][t], acc[dt][t]); \
  } while (0)
#define Q_READ(i) ROWS_READ(i, (u ? aX1 : aX0) + ((i) & 3) * 8192 + qt_ * 2048)
    SYNC(0); Q_READ(0); ROWS_MM();
    SYNC(1); Q_READ(1); ROWS_MM();
    SYNC(2); Q_READ(2); ROWS_MM();
    SYNC(3); Q_READ(3); ROWS_MM();
    SYNC(4); Q_READ(4); ROWS_MM();
    SYNC(5); Q_READ(5); ROWS_MM();
    unsigned aFw = L0 + AUX + w * 4096 + ((g >> 1) * 16 + r) * 16 + 8 * (g & 1);
    OPAQUE(aFw);
#define TILE_MM(dt)                                                                                   \
  do {                                                                                                \
    _Pragma("unroll") for (int u = 0; u < 2; ++u)                                                     \
        _Pragma("unroll") for (int t = 0; t < 4; ++t) acc[dt][t] = mfma(fa[u][dt], fb[u][t], acc[dt][t]); \
  } while (0)
#define TILE_WRITE(dt)                                                                                \
  do {                                                                                                \
    _Pragma("unroll") for (int t = 0; t < 4; ++t) {                                                   \
      u32x2 v = {pack2(acc[dt][t][0], acc[dt][t][1]), pack2(acc[dt][t][2], acc[dt][t][3])};           \
      *(LAS u32x2*)(aFw + t * 1024 + dt * 512) = v;                                                   \
    }                                                                                                 \
  } while (0)
#define HAND_OFF() do { PIN(); TILE_MM(0); PIN(); TILE_MM(1); TILE_WRITE(0); PIN(); TILE_WRITE(1); } while (0)
    SYNC(6); Q_READ(6); MID(); HAND_OFF();

    constexpr float CEXP = 1.4426950408889634f / 16.0f;
    const unsigned kh = w & 1, qt = w >> 1, qh = w >> 2, cq = w & 3;
    f32x4 sc[8][2];
    float mrow, lsum = 0.f, linv[2];
    unsigned aOwn = aLin + qt * 1024, aSr = L0 + STAT + r * 8, aSw = aSr + qt * 128;
    OPAQUE(aOwn); OPAQUE(aSr); OPAQUE(aSw);
    {
      bf16x8 bq[8];
      const unsigned aK = L0 + (32 * kh + r) * 512 + 16 * (g ^ ((r & 8) | ((r & 3) << 1)));
#define K_READ(i, c)                                                                                  \
  do {                                                                                                \
    unsigned a_ = (aK ^ ((c) * 64)) + ((i) & 2) * SLOT;                                               \
    OPAQUE(a_);                                                                                       \
    _Pragma("unroll") for (int v = 0; v < 2; ++v)                                                     \
        _Pragma("unroll") for (int kt = 0; kt < 2; ++kt)                                              \
            ka[(c) & 1][v][kt] = lds_b128(a_ + ((i) & 1) * SLOT + kt * 8192 + v * 256);               \
  } while (0)
#define S_MM(i, c)                                                                                    \
  do {                                                                                                \
    _Pragma("unroll") for (int v = 0; v < 2; ++v)                                                     \
        _Pragma("unroll") for (int kt = 0; kt < 2; ++kt)                                              \
            sc[(i) - 7][kt] = mfma(ka[(c) & 1][v][kt], bq[(c) + 4 * v], sc[(i) - 7][kt]);             \
  } while (0)
#define MAX8(j) fmaxf(fmaxf(fmaxf(sc[j][0][0], sc[j][0][1]), fmaxf(sc[j][0][2], sc[j][0][3])),        \
                      fmaxf(fmaxf(sc[j][1][0], sc[j][1][1]), fmaxf(sc[j][1][2], sc[j][1][3])))
#define S_STEP(i)                                                                                     \
  do {                                                                                                \
    bf16x8 ka[2][2][2];                                                                               \
    sc[(i) - 7][0] = sc[(i) - 7][1] = (f32x4){0.f, 0.f, 0.f, 0.f};                                    \
    K_READ(i, 0); K_READ(i, 1); PIN();                                                                \
    if ((i) == 8) mrow = MAX8(0);                                                                     \
    if ((i) > 8) mrow = fmaxf(mrow, MAX8((i) > 8 ? (i) - 8 : 0));                                     \
    if ((i) >= 8) OPAQUE(mrow);                                                                       \
    S_MM(i, 0); PIN(); K_READ(i, 2); S_MM(i, 1); PIN(); K_READ(i, 3); S_MM(i, 2); PIN(); S_MM(i, 3);  \
  } while (0)
      SYNC(7);
#pragma unroll
      for (int ks = 0; ks < 8; ++ks) bq[ks] = lds_b128(aOwn + ks * 4096);
      S_STEP(7);
      SYNC(8);  S_STEP(8);
      SYNC(9);  S_STEP(9);
      SYNC(10); S_STEP(10);
      SYNC(11); S_STEP(11);
      SYNC(12); S_STEP(12);
      SYNC(13); S_STEP(13);
      SYNC(14); S_STEP(14);
    }
    *(LAS float*)(aSw + kh * 4) = xg_max(fmaxf(mrow, MAX8(7)));
    MID();
    {
      const f32x2 mm = *(LAS f32x2*)(aSw);
      mrow = fmaxf(mm[0], mm[1]) * CEXP;
    }

#define P_WRITE(j)                                                                                    \
  do {                                                                                                \
    f32x4 e_[2];                                                                                      \
    _Pragma("unroll") for (int t = 0; t < 2; ++t)                                                     \
        _Pragma("unroll") for (int c = 0; c < 4; ++c) e_[t][c] = __builtin_amdgcn_exp2f(fmaf(sc[j][t][c], CEXP, -mrow)); \
    lsum += ((e_[0][0] + e_[0][1]) + (e_[0][2] + e_[0][3])) + ((e_[1][0] + e_[1][1]) + (e_[1][2] + e_[1][3])); \
    OPAQUE(lsum);                                                                                     \
    u32x4 v = {pack2(e_[0][0], e_[0][1]), pack2(e_[0][2], e_[0][3]), pack2(e_[1][0], e_[1][1]), pack2(e_[1][2], e_[1][3])}; \
    *(LAS u32x4*)(aOwn + kh * 4096 + ((j) & 1) * 8192) = v;                                           \
  } while (0)
#define O_READ(i) ROWS_READ(i, aLin + (((i) - 15) & 1) * 8192 + (u * 4 + qt_) * 1024)
#define O_STEP(i) do { O_READ(i); P_WRITE((i) - 14); ROWS_MM(); } while (0)
    P_WRITE(0);
#pragma unroll
    for (int i = 0; i < 2; ++i)
#pragma unroll
      for (int j = 0; j < 4; ++j) acc[i][j] = (f32x4){0.f, 0.f, 0.f, 0.f};
    SYNC(15); O_STEP(15);
    SYNC(16); O_STEP(16);
    SYNC(17); O_STEP(17);
    SYNC(18); O_STEP(18);
    SYNC(19); O_STEP(19);
    SYNC(20); O_STEP(20);
    SYNC(21); O_READ(21); P_WRITE(7);
    *(LAS float*)(aSw + (SUMS - STAT) + kh * 4) = xg_sum(lsum);
    ROWS_MM();
    SYNC(22);
    {
      f32x2 ss[2];
      O_READ(22);
#pragma unroll
      for (int t = 0; t < 2; ++t) ss[t] = *(LAS f32x2*)(aSr + (SUMS - STAT) + (2 * qh + t) * 128);
      MID();
      HAND_OFF();
#pragma unroll
      for (int t = 0; t < 2; ++t) linv[t] = 1.0f / (ss[t][0] + ss[t][1]);
    }

    const unsigned pOff = h * PART_BYTES + (qh * 8 + (r >> 1)) * 3584 + cq * 128 + g * 32 + (r & 1) * 16;
    unsigned aWo[2];
    aWo[0] = L0 + (8 * g + qq) * 128 + ((cq ^ (((qq >> 1) & 1) | ((g & 1) << 1))) * 32) + 8 * p;
    aWo[1] = aWo[0] + 2 * SLOT;
    unsigned aB = aLin + qh * 2048;
    OPAQUE(aWo[0]); OPAQUE(aWo[1]); OPAQUE(aB);
    bf16x8 bf[2][8], wa[2][8];
    f32x4 po[2][2];
#define W_FRAG(i, ks) lds_tr(aWo[((i) >> 1) & 1] + ((i) & 1) * SLOT + (ks) * 4096, aWo[((i) >> 1) & 1] + ((i) & 1) * SLOT + (ks) * 4096 + 512)
#define PUBLISH(i)                                                                                    \
  do {                                                                                                \
    const f32x4 a_ = po[(i) & 1][0] * linv[0], b_ = po[(i) & 1][1] * linv[1];                         \
    u32x4 v = {pack2(a_[0], a_[1]), pack2(a_[2], a_[3]), pack2(b_[0], b_[1]), pack2(b_[2], b_[3])};   \
    __builtin_amdgcn_raw_buffer_store_b128(v, prs, pOff, ROT7((unsigned)((i) - 23)) * 512, 16);       \
  } while (0)
#define W_STEP(i)                                                                                     \
  do {                                                                                                \
    po[(i) & 1][0] = po[(i) & 1][1] = (f32x4){0.f, 0.f, 0.f, 0.f};                                    \
    _Pragma("unroll") for (int ks = 0; ks < 8; ++ks) {                                                \
      PIN();                                                                                          \
      po[(i) & 1][0] = mfma(wa[(i) & 1][ks], bf[0][ks], po[(i) & 1][0]);                              \
      po[(i) & 1][1] = mfma(wa[(i) & 1][ks], bf[1][ks], po[(i) & 1][1]);                              \
      if ((i) > 23 && (i) < 29 && ks < 4) {                                                           \
        wa[((i) + 1) & 1][2 * ks] = W_FRAG((i) + 1, 2 * ks);                                          \
        wa[((i) + 1) & 1][2 * ks + 1] = W_FRAG((i) + 1, 2 * ks + 1);                                  \
      }                                                                                               \
      if ((i) > 23 && ks == 4) PUBLISH((i) - 1);                                                      \
    }                                                                                                 \
    PIN();                                                                                            \
  } while (0)
    SYNC(23);
#pragma unroll
    for (int ks = 0; ks < 8; ++ks) {
      wa[1][ks] = W_FRAG(23, ks);
#pragma unroll
      for (int t = 0; t < 2; ++t) bf[t][ks] = lds_b128(aB + ks * 4096 + t * 1024);
      PIN();
    }
#pragma unroll
    for (int ks = 0; ks < 8; ++ks) wa[0][ks] = W_FRAG(24, ks);
    W_STEP(23);
    SYNC(24); W_STEP(24);
    SYNC(25); W_STEP(25);
    SYNC(26); W_STEP(26);
    SYNC(27); W_STEP(27);
    SYNC(28); W_STEP(28);
    STAMP(57); STAMP(58);
    W_STEP(29);
    PUBLISH(29);
    STAMP(59);
  }

  WAIT_V(0); BAR();
  STAMP(60);
  if (w == 0) {
    gu32* peer = (gu32*)(ws + FLAG_OFF + (grp * 16 + (l & 15)) * FLAG_STRIDE) + h;
    unsigned spins = 0;
    for (;;) {
      if ((spins & 63) == 0 && l < 16) __hip_atomic_store(peer, 1u, RLX_AGENT);
      const unsigned f = __hip_atomic_load(myflags + (l & 15), RLX_AGENT);
      if (__all(f == 1u)) break;
      if (++spins > (1u << 22)) break;
      __builtin_amdgcn_s_sleep(1);
    }
    if (l < 16) __hip_atomic_store(peer, 1u, RLX_AGENT);
  }
  BAR();
  STAMP(61);
  __builtin_amdgcn_fence(__ATOMIC_ACQUIRE, "wavefront");
  if (w < 7) {
    const unsigned half = l >> 5, it = l & 31;
    const unsigned off = half * (8 * PART_BYTES) + h * 3584 + w * 512 + it * 16;
    u32x4 pv[8];
#pragma unroll
    for (int e = 0; e < 8; ++e) pv[e] = __builtin_amdgcn_raw_buffer_load_b128(prs, off + e * PART_BYTES, 0, 16);
    float s[8];
#pragma unroll
    for (int i = 0; i < 8; ++i) s[i] = 0.f;
#pragma unroll
    for (int e = 0; e < 8; ++e)
#pragma unroll
      for (int i = 0; i < 4; ++i) {
        s[2 * i] += __uint_as_float(pv[e][i] << 16);
        s[2 * i + 1] += __uint_as_float(pv[e][i] & 0xffff0000u);
      }
    float o[4];
#pragma unroll
    for (int i = 0; i < 4; ++i) {
      auto sw = __builtin_amdgcn_permlane32_swap(__float_as_uint(s[i]), __float_as_uint(s[4 + i]), false, false);
      o[i] = __uint_as_float(sw[0]) + __uint_as_float(sw[1]);
    }
    const unsigned q = 32 * (h >> 3) + 16 * half + 2 * (h & 7) + (it & 1);
    const unsigned n = 64 * w + 4 * (it >> 1);
    u32x2 v = {pack2(o[0], o[1]), pack2(o[2], o[3])};
    *(u32x2*)(out + (size_t)(b * 64 + q) * 1792 + br * 896 + n * 2) = v;
  }
}

extern "C" void kernel_launch(void* const* d_in, const int* in_sizes, int n_in, void* d_out, int out_size, void* d_ws,
                              size_t ws_size, hipStream_t stream) {
  gqa2_fused<<<256, 768, 0, stream>>>((const char*)d_in[0], (const char*)d_in[1], (const char*)d_in[2], (const char*)d_in[3],
                                      (const char*)d_in[4], (const char*)d_in[5], (const char*)d_in[6], (const char*)d_in[7],
                                      (const char*)d_in[8], (const char*)d_in[9], (char*)d_out, (char*)d_ws);
}
```

```cpp
#include <hip/hip_runtime.h>
#include <hip/hip_bf16.h>
#pragma clang diagnostic ignored "-Wint-to-pointer-cast"

typedef __attribute__((ext_vector_type(8))) __bf16 bf16x8;
typedef __attribute__((ext_vector_type(4))) __bf16 bf16x4;
typedef __attribute__((ext_vector_type(2))) __bf16 bf16x2;
typedef __attribute__((ext_vector_type(4))) float f32x4;
typedef __attribute__((ext_vector_type(2))) float f32x2;
typedef __attribute__((ext_vector_type(4))) unsigned u32x4;
typedef __attribute__((ext_vector_type(2))) unsigned u32x2;
typedef __attribute__((address_space(1))) unsigned gu32;
#define LAS __attribute__((address_space(3)))
#define RLX_AGENT __ATOMIC_RELAXED, __HIP_MEMORY_SCOPE_AGENT

#define DIAG 0

constexpr unsigned SLOT = 32768u;
constexpr unsigned AUX = 4u * SLOT;
constexpr unsigned PBUF = 3u * SLOT;
#define KV_NT true
constexpr unsigned slot_of(int i) {
  return i < 15 ? (i & 3) : i < 22 ? (i - 15) % 3 : i == 22 ? 3 : i == 23 ? 1 : i == 24 ? 2 : i == 25 ? 0 : i == 26 ? 3 : i == 27 ? 1 : i == 28 ? 2 : 0;
}
constexpr unsigned PART_BYTES = 57344u;
constexpr unsigned FLAG_OFF = 256u * PART_BYTES;
constexpr unsigned FLAG_STRIDE = 256u;
constexpr unsigned DIAG_OFF = FLAG_OFF + 256u * FLAG_STRIDE;

#define WAIT_V(n) asm volatile("s_waitcnt vmcnt(" #n ")" ::: "memory")
#define WAIT_L0() asm volatile("s_waitcnt lgkmcnt(0)" ::: "memory")
#define BAR() asm volatile("s_barrier" ::: "memory")

__device__ __forceinline__ unsigned pack2(float a, float b) {
  f32x2 v = {a, b};
  bf16x2 p = __builtin_convertvector(v, bf16x2);
  return __builtin_bit_cast(unsigned, p);
}

#define DMA4_BODY(POL)                                \
      "s_nop 4\n\t"                                   \
      "s_mov_b32 %0, m0\n\t"                          \
      "s_mov_b32 m0, %1\n\t"                          \
      "s_nop 0\n\t"                                   \
      "global_load_lds_dwordx4 %2, %4" POL "\n\t"     \
      "s_add_u32 m0, m0, 0x400\n\t"                   \
      "s_nop 0\n\t"                                   \
      "global_load_lds_dwordx4 %3, %4" POL "\n\t"     \
      "s_add_u32 m0, m0, 0x400\n\t"                   \
      "s_nop 0\n\t"                                   \
      "global_load_lds_dwordx4 %2, %5" POL "\n\t"     \
      "s_add_u32 m0, m0, 0x400\n\t"                   \
      "s_nop 0\n\t"                                   \
      "global_load_lds_dwordx4 %3, %5" POL "\n\t"     \
      "s_mov_b32 m0, %0"
template <bool NT>
__device__ __forceinline__ void dma4(unsigned lds, unsigned va, unsigned vb, const char* g0, const char* g1) {
  unsigned keep;
  if (NT)
    asm volatile(DMA4_BODY(" nt") : "=&s"(keep) : "s"(lds), "v"(va), "v"(vb), "s"(g0), "s"(g1) : "memory", "scc");
  else
    asm volatile(DMA4_BODY("") : "=&s"(keep) : "s"(lds), "v"(va), "v"(vb), "s"(g0), "s"(g1) : "memory", "scc");
}
__device__ __forceinline__ void dma1(unsigned lds, unsigned va, const char* g0) {
  unsigned keep;
  asm volatile(
      "s_nop 4\n\t"
      "s_mov_b32 %0, m0\n\t"
      "s_mov_b32 m0, %1\n\t"
      "s_nop 0\n\t"
      "global_load_lds_dwordx4 %2, %3\n\t"
      "s_mov_b32 m0, %0"
      : "=&s"(keep)
      : "s"(lds), "v"(va), "s"(g0)
      : "memory");
}

__device__ __forceinline__ bf16x8 lds_b128(unsigned addr) { return *(LAS bf16x8*)(addr); }
__device__ __forceinline__ bf16x8 lds_tr(unsigned a0, unsigned a1) {
  bf16x4 lo = __builtin_amdgcn_ds_read_tr16_b64_v4bf16((LAS bf16x4*)(a0));
  bf16x4 hi = __builtin_amdgcn_ds_read_tr16_b64_v4bf16((LAS bf16x4*)(a1));
  return __builtin_shufflevector(lo, hi, 0, 1, 2, 3, 4, 5, 6, 7);
}
__device__ __forceinline__ f32x4 mfma(bf16x8 a, bf16x8 b, f32x4 c) {
  return __builtin_amdgcn_mfma_f32_16x16x32_bf16(a, b, c, 0, 0, 0);
}
__device__ __forceinline__ float xg_max(float v) {
  auto a = __builtin_amdgcn_permlane16_swap(__float_as_uint(v), __float_as_uint(v), false, false);
  v = fmaxf(__uint_as_float(a[0]), __uint_as_float(a[1]));
  auto b = __builtin_amdgcn_permlane32_swap(__float_as_uint(v), __float_as_uint(v), false, false);
  return fmaxf(__uint_as_float(b[0]), __uint_as_float(b[1]));
}
__device__ __forceinline__ float xg_sum(float v) {
  auto a = __builtin_amdgcn_permlane16_swap(__float_as_uint(v), __float_as_uint(v), false, false);
  v = __uint_as_float(a[0]) + __uint_as_float(a[1]);
  auto b = __builtin_amdgcn_permlane32_swap(__float_as_uint(v), __float_as_uint(v), false, false);
  return __uint_as_float(b[0]) + __uint_as_float(b[1]);
}

#define STAMP(i) do { } while (0)

__global__ __launch_bounds__(512, 2) void gqa2_fused(
    const char* __restrict__ xA, const char* __restrict__ kA, const char* __restrict__ vA, const char* __restrict__ wqA,
    const char* __restrict__ woA, const char* __restrict__ xB, const char* __restrict__ kB, const char* __restrict__ vB,
    const char* __restrict__ wqB, const char* __restrict__ woB, char* __restrict__ out, char* ws) {
  __shared__ __attribute__((aligned(16))) char smem[163840];
  const unsigned L0 = (unsigned)(size_t)(LAS char*)smem;

  const unsigned tid = threadIdx.x;
  const unsigned w = __builtin_amdgcn_readfirstlane(tid >> 6);
  const unsigned l = tid & 63, r = l & 15, g = l >> 4, qq = (l & 15) >> 2, p = l & 3;

  const unsigned bid = blockIdx.x, xcd = bid & 7, idx = bid >> 3;
  const unsigned br = xcd >> 2, b = idx & 7, h = (xcd & 3) * 4 + (idx >> 3), kvh = h >> 1;
  const unsigned rotw = b == 7 ? 0 : b;
  const unsigned grp = br * 8 + b;

  const char* xg = (br ? xB : xA) + (size_t)b * (64 * 896);
  const char* wq = (br ? wqB : wqA) + h * 512;
  const char* kg = (br ? kB : kA) + (size_t)(b * 8 + kvh) * (512 * 512);
  const char* vg = (br ? vB : vA) + (size_t)(b * 8 + kvh) * (512 * 512);
  const char* wo = (br ? woB : woA) + (size_t)h * (256 * 896);

  gu32* myflags = (gu32*)(ws + FLAG_OFF + (grp * 16 + h) * FLAG_STRIDE);
  if (tid < 16) __hip_atomic_store(myflags + tid, 0u, RLX_AGENT);

  const unsigned hi = l >> 5;
  const unsigned swA = 16 * ((l & 31) ^ ((hi | ((w & 1) << 2)) << 1));
  const unsigned swB = 16 * ((l & 31) ^ ((hi | 2 | ((w & 1) << 2)) << 1));
  const unsigned vWqA = hi * 8192 + swA, vWqB = (2 + hi) * 8192 + swB;
  const unsigned vRA = hi * 512 + swA, vRB = (2 + hi) * 512 + swB;
  const unsigned vX = (l >> 3) * 896 + 16 * ((l & 7) ^ ((4 * w + (l >> 4)) & 7));
  const unsigned vWoA = (l >> 3) * 896 + 16 * ((l & 7) ^ (((l >> 4) & 1) << 1));
  const unsigned vWoB = (8 + (l >> 3)) * 896 + 16 * ((l & 7) ^ ((((l >> 4) & 1) | 2) << 1));
  const char* wqw = wq + (size_t)w * (8 * 8192);
  const char* xw = xg + w * (8 * 896);
  const char* kw = kg + w * (16 * 512);
  const char* vw = vg + w * (8 * 512);
  const char* wow = wo + w * (32 * 896);
#define ROT7(s) ((s) + rotw >= 7 ? (s) + rotw - 7 : (s) + rotw)
#define ISSUE(i)                                                                                      \
  do {                                                                                                \
    const unsigned dst_ = L0 + slot_of(i) * SLOT + w * 4096;                                          \
    if ((i) < 7) {                                                                                    \
      const unsigned s_ = ROT7((unsigned)(i));                                                        \
      const char* gb_ = wqw + (size_t)s_ * (64 * 8192);                                               \
      dma4<false>(dst_, vWqA, vWqB, gb_, gb_ + 4 * 8192);                                             \
      dma1(L0 + AUX + ((i) & 3) * 8192 + w * 1024, vX, xw + s_ * 128);                                \
    } else if ((i) < 15) {                                                                            \
      const char* gb_ = kw + ((((i) - 7) >> 1) * 128 + (((i) - 7) & 1) * 4) * 512;                    \
      dma4<KV_NT>(dst_, vRA, vRB, gb_, gb_ + 8 * 512);                                                \
    } else if ((i) < 23) {                                                                            \
      const char* gb_ = vw + ((i) - 15) * (64 * 512);                                                 \
      dma4<KV_NT>(dst_, vRA, vRB, gb_, gb_ + 4 * 512);                                                \
    } else {                                                                                          \
      const char* gb_ = wow + ROT7((unsigned)((i) - 23)) * 128;                                       \
      dma4<false>(dst_, vWoA, vWoB, gb_, gb_ + 16 * 896);                                             \
    }                                                                                                 \
  } while (0)
#define SYNC(i, n) do { if ((i) > 0) STAMP(2 * (i) - 1); WAIT_L0(); WAIT_V(n); BAR(); STAMP(2 * (i)); } while (0)

  ISSUE(0); BAR();
  ISSUE(1); BAR();
  ISSUE(2); BAR();
  ISSUE(3);

  f32x4 acc[2][4];
#pragma unroll
  for (int i = 0; i < 2; ++i)
#pragma unroll
    for (int j = 0; j < 4; ++j) acc[i][j] = (f32x4){0.f, 0.f, 0.f, 0.f};
  const unsigned aT = L0 + (8 * g + qq) * 512 + (((2 * w) ^ (qq | ((g & 1) << 2))) * 32) + 8 * p;
  const unsigned aX0 = L0 + AUX + r * 128 + 16 * (g ^ (r >> 1));
  const unsigned aLin = L0 + l * 16;
#define ROWS_COMPUTE(i, BADDR)                                                                        \
  do {                                                                                                \
    _Pragma("unroll") for (int u = 0; u < 2; ++u) {                                                   \
      bf16x8 fb[4], fa[2];                                                                            \
      _Pragma("unroll") for (int qt = 0; qt < 4; ++qt) fb[qt] = lds_b128(BADDR);                      \
      _Pragma("unroll") for (int dt = 0; dt < 2; ++dt) {                                              \
        const unsigned a_ = (aT ^ (dt * 32)) + slot_of(i) * SLOT + u * 16384;                         \
        fa[dt] = lds_tr(a_, a_ + 2048);                                                               \
      }                                                                                               \
      _Pragma("unroll") for (int dt = 0; dt < 2; ++dt)                                                \
          _Pragma("unroll") for (int qt = 0; qt < 4; ++qt) acc[dt][qt] = mfma(fa[dt], fb[qt], acc[dt][qt]); \
    }                                                                                                 \
  } while (0)
#define Q_COMPUTE(i) ROWS_COMPUTE(i, (aX0 ^ (u * 64)) + ((i) & 3) * 8192 + qt * 2048)

  SYNC(0, 15); Q_COMPUTE(0);
  SYNC(1, 10); ISSUE(4); Q_COMPUTE(1);
  SYNC(2, 10); ISSUE(5); Q_COMPUTE(2);
  SYNC(3, 10); ISSUE(6); Q_COMPUTE(3);
  SYNC(4, 10); ISSUE(7); Q_COMPUTE(4);
  SYNC(5, 9);  ISSUE(8); Q_COMPUTE(5);
  SYNC(6, 8);  ISSUE(9); Q_COMPUTE(6);
  WAIT_L0(); BAR();

  const unsigned aFw = L0 + AUX + w * 4096 + ((g >> 1) * 16 + r) * 16 + 8 * (g & 1);
#pragma unroll
  for (int dt = 0; dt < 2; ++dt)
#pragma unroll
    for (int qt = 0; qt < 4; ++qt) {
      u32x2 v = {pack2(acc[dt][qt][0], acc[dt][qt][1]), pack2(acc[dt][qt][2], acc[dt][qt][3])};
      *(LAS u32x2*)(aFw + qt * 1024 + dt * 512) = v;
    }

  const unsigned qh = w >> 2, cq = w & 3;
  constexpr float CEXP = 1.4426950408889634f / 16.0f;
  bf16x8 bf[2][8];
  f32x4 e[8][2];
  float ml[8][2], ls[8][2];
  const unsigned aK = L0 + (16 * cq + r) * 512 + 16 * (g ^ ((r & 8) | ((r & 3) << 1)));
#define LOAD_BF()                                                                                     \
  do {                                                                                                \
    _Pragma("unroll") for (int ks = 0; ks < 8; ++ks)                                                  \
        _Pragma("unroll") for (int qt = 0; qt < 2; ++qt)                                              \
            bf[qt][ks] = lds_b128(aLin + AUX + qh * 2048 + ks * 4096 + qt * 1024);                    \
  } while (0)
#define S_COMPUTE(i)                                                                                  \
  do {                                                                                                \
    f32x4 s0_ = {0.f, 0.f, 0.f, 0.f}, s1_ = {0.f, 0.f, 0.f, 0.f};                                     \
    _Pragma("unroll") for (int ks = 0; ks < 8; ++ks) {                                                \
      const bf16x8 ka = lds_b128((aK ^ ((ks & 3) * 64)) + slot_of(i) * SLOT + (ks >> 2) * 256);       \
      s0_ = mfma(ka, bf[0][ks], s0_);                                                                 \
      s1_ = mfma(ka, bf[1][ks], s1_);                                                                 \
    }                                                                                                 \
    _Pragma("unroll") for (int qt = 0; qt < 2; ++qt) {                                                \
      const f32x4 s_ = qt ? s1_ : s0_;                                                                \
      const float m_ = fmaxf(fmaxf(s_[0], s_[1]), fmaxf(s_[2], s_[3])) * CEXP;                        \
      _Pragma("unroll") for (int c = 0; c < 4; ++c)                                                   \
          e[(i) - 7][qt][c] = __builtin_amdgcn_exp2f(fmaf(s_[c], CEXP, -m_));                         \
      ml[(i) - 7][qt] = m_;                                                                           \
      ls[(i) - 7][qt] = (e[(i) - 7][qt][0] + e[(i) - 7][qt][1]) + (e[(i) - 7][qt][2] + e[(i) - 7][qt][3]); \
    }                                                                                                 \
  } while (0)

  SYNC(7, 8); ISSUE(10); LOAD_BF(); S_COMPUTE(7);
  SYNC(8, 8); ISSUE(11); S_COMPUTE(8);
  SYNC(9, 8); ISSUE(12); S_COMPUTE(9);
  SYNC(10, 8); ISSUE(13); S_COMPUTE(10);
  SYNC(11, 8); ISSUE(14); S_COMPUTE(11);
  SYNC(12, 8); S_COMPUTE(12);
  SYNC(13, 4); ISSUE(15); S_COMPUTE(13);
  SYNC(14, 4); ISSUE(16); S_COMPUTE(14);

#pragma unroll
  for (int qt = 0; qt < 2; ++qt) {
    float m = ml[0][qt];
#pragma unroll
    for (int s = 1; s < 8; ++s) m = fmaxf(m, ml[s][qt]);
    m = xg_max(m);
    *(LAS float*)(L0 + PBUF + (g * 4 + 2 * qh) * 1024 + (qt * 16 + r) * 16 + cq * 4) = m;
  }
  WAIT_L0(); BAR();
  ISSUE(17);
  float rs[2];
  {
    f32x4 mm[2];
#pragma unroll
    for (int qt = 0; qt < 2; ++qt) mm[qt] = *(LAS f32x4*)(L0 + PBUF + (cq * 4 + 2 * qh) * 1024 + (qt * 16 + r) * 16);
    WAIT_L0();
#pragma unroll
    for (int qt = 0; qt < 2; ++qt) {
      const float m = fmaxf(fmaxf(mm[qt][0], mm[qt][1]), fmaxf(mm[qt][2], mm[qt][3]));
      float sum = 0.f;
#pragma unroll
      for (int u = 0; u < 4; ++u) {
        const float f0 = __builtin_amdgcn_exp2f(ml[2 * u][qt] - m), f1 = __builtin_amdgcn_exp2f(ml[2 * u + 1][qt] - m);
        sum = fmaf(f0, ls[2 * u][qt], fmaf(f1, ls[2 * u + 1][qt], sum));
        const f32x4 a = e[2 * u][qt] * f0, c = e[2 * u + 1][qt] * f1;
        u32x4 v = {pack2(a[0], a[1]), pack2(a[2], a[3]), pack2(c[0], c[1]), pack2(c[2], c[3])};
        *(LAS u32x4*)(aLin + PBUF + ((4 * u + cq) * 4 + 2 * qh + qt) * 1024) = v;
      }
      rs[qt] = xg_sum(sum);
    }
  }

#pragma unroll
  for (int i = 0; i < 2; ++i)
#pragma unroll
    for (int j = 0; j < 4; ++j) acc[i][j] = (f32x4){0.f, 0.f, 0.f, 0.f};
#define O_COMPUTE(i) ROWS_COMPUTE(i, aLin + PBUF + ((((i) - 15) * 2 + u) * 4 + qt) * 1024)
  float linv[4];
  SYNC(15, 8); O_COMPUTE(15);
  SYNC(16, 4); ISSUE(18);
  if (g < 2) *(LAS float*)(L0 + PBUF + (32 * qh + l) * 16 + cq * 4) = g ? rs[1] : rs[0];
  O_COMPUTE(16);
  SYNC(17, 4); ISSUE(19);
#pragma unroll
  for (int qt = 0; qt < 4; ++qt) {
    const f32x4 s = *(LAS f32x4*)(L0 + PBUF + (16 * qt + r) * 16);
    linv[qt] = 1.0f / ((s[0] + s[1]) + (s[2] + s[3]));
  }
  O_COMPUTE(17);
  SYNC(18, 4); ISSUE(20); O_COMPUTE(18);
  SYNC(19, 4); ISSUE(21); ISSUE(22); O_COMPUTE(19);
  SYNC(20, 8); ISSUE(23); O_COMPUTE(20);
  SYNC(21, 8); ISSUE(24); O_COMPUTE(21);
  SYNC(22, 8); ISSUE(25); O_COMPUTE(22);
  WAIT_L0(); BAR();
  ISSUE(26);
#pragma unroll
  for (int dt = 0; dt < 2; ++dt)
#pragma unroll
    for (int qt = 0; qt < 4; ++qt) {
      const f32x4 o = acc[dt][qt] * linv[qt];
      u32x2 v = {pack2(o[0], o[1]), pack2(o[2], o[3])};
      *(LAS u32x2*)(aFw + qt * 1024 + dt * 512) = v;
    }

  const __amdgpu_buffer_rsrc_t prs = __builtin_amdgcn_make_buffer_rsrc(ws + (size_t)grp * (16 * PART_BYTES), 0, 16 * PART_BYTES, 0x00020000);
  const unsigned pOff = h * PART_BYTES + (qh * 8 + (r >> 1)) * 3584 + cq * 128 + g * 32 + (r & 1) * 16;
  const unsigned aWo = L0 + (8 * g + qq) * 128 + ((cq ^ (((qq >> 1) & 1) | ((g & 1) << 1))) * 32) + 8 * p;
#define W_COMPUTE(i)                                                                                  \
  do {                                                                                                \
    f32x4 s0_ = {0.f, 0.f, 0.f, 0.f}, s1_ = {0.f, 0.f, 0.f, 0.f};                                     \
    _Pragma("unroll") for (int ks = 0; ks < 8; ++ks) {                                                \
      const unsigned a_ = aWo + slot_of(i) * SLOT + ks * 4096;                                        \
      const bf16x8 wa = lds_tr(a_, a_ + 512);                                                         \
      s0_ = mfma(wa, bf[0][ks], s0_);                                                                 \
      s1_ = mfma(wa, bf[1][ks], s1_);                                                                 \
    }                                                                                                 \
    u32x4 v = {pack2(s0_[0], s0_[1]), pack2(s0_[2], s0_[3]), pack2(s1_[0], s1_[1]), pack2(s1_[2], s1_[3])}; \
    __builtin_amdgcn_raw_buffer_store_b128(v, prs, pOff, ROT7((unsigned)((i) - 23)) * 512, 16);       \
  } while (0)
  SYNC(23, 12); LOAD_BF(); W_COMPUTE(23);
  SYNC(24, 9);  ISSUE(27); W_COMPUTE(24);
  SYNC(25, 10); ISSUE(28); W_COMPUTE(25);
  SYNC(26, 11); ISSUE(29); W_COMPUTE(26);
  SYNC(27, 11); W_COMPUTE(27);
  SYNC(28, 7);  W_COMPUTE(28);
  SYNC(29, 3);  W_COMPUTE(29);
  STAMP(59);

  WAIT_V(0); BAR();
  STAMP(60);
  if (w == 0) {
    gu32* peer = (gu32*)(ws + FLAG_OFF + (grp * 16 + (l & 15)) * FLAG_STRIDE) + h;
    unsigned spins = 0;
    for (;;) {
      if ((spins & 63) == 0 && l < 16) __hip_atomic_store(peer, 1u, RLX_AGENT);
      const unsigned f = __hip_atomic_load(myflags + (l & 15), RLX_AGENT);
      if (__all(f == 1u)) break;
      if (++spins > (1u << 22)) break;
      __builtin_amdgcn_s_sleep(1);
    }
    if (l < 16) __hip_atomic_store(peer, 1u, RLX_AGENT);
  }
  BAR();
  STAMP(61);
  __builtin_amdgcn_fence(__ATOMIC_ACQUIRE, "wavefront");
  if (w < 7) {
    const unsigned half = l >> 5, it = l & 31;
    const unsigned off = half * (8 * PART_BYTES) + h * 3584 + w * 512 + it * 16;
    u32x4 pv[8];
#pragma unroll
    for (int e = 0; e < 8; ++e) pv[e] = __builtin_amdgcn_raw_buffer_load_b128(prs, off + e * PART_BYTES, 0, 16);
    float s[8];
#pragma unroll
    for (int i = 0; i < 8; ++i) s[i] = 0.f;
#pragma unroll
    for (int e = 0; e < 8; ++e)
#pragma unroll
      for (int i = 0; i < 4; ++i) {
        s[2 * i] += __uint_as_float(pv[e][i] << 16);
        s[2 * i + 1] += __uint_as_float(pv[e][i] & 0xffff0000u);
      }
    float o[4];
#pragma unroll
    for (int i = 0; i < 4; ++i) {
      auto sw = __builtin_amdgcn_permlane32_swap(__float_as_uint(s[i]), __float_as_uint(s[4 + i]), false, false);
      o[i] = __uint_as_float(sw[0]) + __uint_as_float(sw[1]);
    }
    const unsigned q = 32 * (h >> 3) + 16 * half + 2 * (h & 7) + (it & 1);
    const unsigned n = 64 * w + 4 * (it >> 1);
    u32x2 v = {pack2(o[0], o[1]), pack2(o[2], o[3])};
    *(u32x2*)(out + (size_t)(b * 64 + q) * 1792 + br * 896 + n * 2) = v;
  }
}

extern "C" void kernel_launch(void* const* d_in, const int* in_sizes, int n_in, void* d_out, int out_size, void* d_ws,
                              size_t ws_size, hipStream_t stream) {
  gqa2_fused<<<256, 512, 0, stream>>>((const char*)d_in[0], (const char*)d_in[1], (const char*)d_in[2], (const char*)d_in[3],
                                      (const char*)d_in[4], (const char*)d_in[5], (const char*)d_in[6], (const char*)d_in[7],
                                      (const char*)d_in[8], (const char*)d_in[9], (char*)d_out, (char*)d_ws);
}
```

```cpp
#include <hip/hip_runtime.h>
#include <hip/hip_bf16.h>
#pragma clang diagnostic ignored "-Wint-to-pointer-cast"

typedef __attribute__((ext_vector_type(8))) __bf16 bf16x8;
typedef __attribute__((ext_vector_type(4))) __bf16 bf16x4;
typedef __attribute__((ext_vector_type(2))) __bf16 bf16x2;
typedef __attribute__((ext_vector_type(4))) float f32x4;
typedef __attribute__((ext_vector_type(2))) float f32x2;
typedef __attribute__((ext_vector_type(4))) unsigned u32x4;
typedef __attribute__((ext_vector_type(2))) unsigned u32x2;
typedef __attribute__((address_space(1))) unsigned gu32;
#define LAS __attribute__((address_space(3)))
#define RLX_AGENT __ATOMIC_RELAXED, __HIP_MEMORY_SCOPE_AGENT

#define DIAG 0

constexpr unsigned SLOT = 32768u;
constexpr unsigned AUX = 4u * SLOT;
constexpr unsigned STAT = AUX + 16384u;
constexpr unsigned SUMS = AUX + 17408u;
#define KV_NT true
constexpr unsigned PART_BYTES = 57344u;
constexpr unsigned FLAG_OFF = 256u * PART_BYTES;
constexpr unsigned FLAG_STRIDE = 256u;
constexpr unsigned DIAG_OFF = FLAG_OFF + 256u * FLAG_STRIDE;

#define WAIT_V(n) asm volatile("s_waitcnt vmcnt(" #n ")" ::: "memory")
#define WAIT_L0() do { asm volatile("" ::: "memory"); __builtin_amdgcn_s_waitcnt(0xC07F); asm volatile("" ::: "memory"); } while (0)
#define BAR() asm volatile("s_barrier" ::: "memory")

__device__ __forceinline__ unsigned pack2(float a, float b) {
  f32x2 v = {a, b};
  bf16x2 p = __builtin_convertvector(v, bf16x2);
  return __builtin_bit_cast(unsigned, p);
}

#define DMA4_BODY(POL)                                \
      "s_nop 4\n\t"                                   \
      "s_mov_b32 %0, m0\n\t"                          \
      "s_mov_b32 m0, %1\n\t"                          \
      "s_nop 0\n\t"                                   \
      "global_load_lds_dwordx4 %2, %4" POL "\n\t"     \
      "s_add_u32 m0, m0, 0x400\n\t"                   \
      "s_nop 0\n\t"                                   \
      "global_load_lds_dwordx4 %3, %4" POL "\n\t"     \
      "s_add_u32 m0, m0, 0x400\n\t"                   \
      "s_nop 0\n\t"                                   \
      "global_load_lds_dwordx4 %2, %5" POL "\n\t"     \
      "s_add_u32 m0, m0, 0x400\n\t"                   \
      "s_nop 0\n\t"                                   \
      "global_load_lds_dwordx4 %3, %5" POL "\n\t"     \
      "s_mov_b32 m0, %0"
template <bool NT>
__device__ __forceinline__ void dma4(unsigned lds, unsigned va, unsigned vb, const char* g0, const char* g1) {
  unsigned keep;
  if (NT)
    asm volatile(DMA4_BODY(" nt") : "=&s"(keep) : "s"(lds), "v"(va), "v"(vb), "s"(g0), "s"(g1) : "memory", "scc");
  else
    asm volatile(DMA4_BODY("") : "=&s"(keep) : "s"(lds), "v"(va), "v"(vb), "s"(g0), "s"(g1) : "memory", "scc");
}
__device__ __forceinline__ void dma1(unsigned lds, unsigned va, const char* g0) {
  unsigned keep;
  asm volatile(
      "s_nop 4\n\t"
      "s_mov_b32 %0, m0\n\t"
      "s_mov_b32 m0, %1\n\t"
      "s_nop 0\n\t"
      "global_load_lds_dwordx4 %2, %3\n\t"
      "s_mov_b32 m0, %0"
      : "=&s"(keep)
      : "s"(lds), "v"(va), "s"(g0)
      : "memory");
}

__device__ __forceinline__ bf16x8 lds_b128(unsigned addr) { return *(LAS bf16x8*)(addr); }
__device__ __forceinline__ bf16x8 lds_tr(unsigned a0, unsigned a1) {
  bf16x4 lo = __builtin_amdgcn_ds_read_tr16_b64_v4bf16((LAS bf16x4*)(a0));
  bf16x4 hi = __builtin_amdgcn_ds_read_tr16_b64_v4bf16((LAS bf16x4*)(a1));
  return __builtin_shufflevector(lo, hi, 0, 1, 2, 3, 4, 5, 6, 7);
}
__device__ __forceinline__ f32x4 mfma(bf16x8 a, bf16x8 b, f32x4 c) {
  return __builtin_amdgcn_mfma_f32_16x16x32_bf16(a, b, c, 0, 0, 0);
}
__device__ __forceinline__ float xg_max(float v) {
  auto a = __builtin_amdgcn_permlane16_swap(__float_as_uint(v), __float_as_uint(v), false, false);
  v = fmaxf(__uint_as_float(a[0]), __uint_as_float(a[1]));
  auto b = __builtin_amdgcn_permlane32_swap(__float_as_uint(v), __float_as_uint(v), false, false);
  return fmaxf(__uint_as_float(b[0]), __uint_as_float(b[1]));
}
__device__ __forceinline__ float xg_sum(float v) {
  auto a = __builtin_amdgcn_permlane16_swap(__float_as_uint(v), __float_as_uint(v), false, false);
  v = __uint_as_float(a[0]) + __uint_as_float(a[1]);
  auto b = __builtin_amdgcn_permlane32_swap(__float_as_uint(v), __float_as_uint(v), false, false);
  return __uint_as_float(b[0]) + __uint_as_float(b[1]);
}

#define STAMP(i) do { } while (0)

__global__ __launch_bounds__(768, 3) void gqa2_fused(
    const char* __restrict__ xA, const char* __restrict__ kA, const char* __restrict__ vA, const char* __restrict__ wqA,
    const char* __restrict__ woA, const char* __restrict__ xB, const char* __restrict__ kB, const char* __restrict__ vB,
    const char* __restrict__ wqB, const char* __restrict__ woB, char* __restrict__ out, char* ws) {
  __shared__ __attribute__((aligned(16))) char smem[163840];
  const unsigned L0 = (unsigned)(size_t)(LAS char*)smem;

  const unsigned tid = threadIdx.x;
  const unsigned w = __builtin_amdgcn_readfirstlane(tid >> 6);
  const unsigned l = tid & 63, r = l & 15, g = l >> 4, qq = (l & 15) >> 2, p = l & 3;

  const unsigned bid = blockIdx.x, xcd = bid & 7, idx = bid >> 3;
  const unsigned br = xcd >> 2, b = idx & 7, h = (xcd & 3) * 4 + (idx >> 3), kvh = h >> 1;
  const unsigned rotw = (b + 2 * (h & 3)) % 7;
  const unsigned grp = br * 8 + b;

  const char* xg = (br ? xB : xA) + (size_t)b * (64 * 896);
  const char* wq = (br ? wqB : wqA) + h * 512;
  const char* kg = (br ? kB : kA) + (size_t)(b * 8 + kvh) * (512 * 512);
  const char* vg = (br ? vB : vA) + (size_t)(b * 8 + kvh) * (512 * 512);
  const char* wo = (br ? woB : woA) + (size_t)h * (256 * 896);

  gu32* myflags = (gu32*)(ws + FLAG_OFF + (grp * 16 + h) * FLAG_STRIDE);
  if (tid < 16) __hip_atomic_store(myflags + tid, 0u, RLX_AGENT);

#define ROT7(s) ((s) + rotw >= 7 ? (s) + rotw - 7 : (s) + rotw)
#define PIN() __builtin_amdgcn_sched_barrier(0)
#define OPAQUE(x) asm volatile("" : "+v"(x))
  const __amdgpu_buffer_rsrc_t prs = __builtin_amdgcn_make_buffer_rsrc(ws + (size_t)grp * (16 * PART_BYTES), 0, 16 * PART_BYTES, 0x00020000);

  if (w >= 8) {
#define SWP b
    const unsigned swp = SWP;
    const unsigned j2 = 2 * (w - 8), hi = l >> 5;
    unsigned vWq[2][2], vR[2][2], vX[2];
#pragma unroll
    for (int e = 0; e < 2; ++e) {
#pragma unroll
      for (int c = 0; c < 2; ++c) {
        const unsigned sw = 16 * ((l & 31) ^ ((hi | (2 * c) | (e << 2)) << 1));
        vWq[e][c] = (2 * c + hi) * 8192 + sw;
        vR[e][c] = (2 * c + hi) * 512 + sw;
      }
      vX[e] = (l >> 3) * 896 + 16 * ((l & 7) ^ ((4 * e + (l >> 4)) & 7));
    }
    const unsigned vWo[2] = {(l >> 3) * 896 + 16 * ((l & 7) ^ (((l >> 4) & 1) << 1)),
                             (8 + (l >> 3)) * 896 + 16 * ((l & 7) ^ ((((l >> 4) & 1) | 2) << 1))};
    const char* wqw = wq + (size_t)j2 * (8 * 8192);
    const char* xw = xg + j2 * (8 * 896);
    const char* kw = kg + (32 * (j2 >> 2) + 4 * ((j2 >> 1) & 1)) * 512;
    const char* vw = vg + j2 * (8 * 512);
    const char* wow = wo + j2 * (32 * 896);
#define HALF(i, e)                                                                                    \
  do {                                                                                                \
    const unsigned dst_ = L0 + ((i) & 3) * SLOT + (j2 + (e)) * 4096;                                  \
    if ((i) < 7) {                                                                                    \
      const unsigned s_ = ROT7((unsigned)(i));                                                        \
      const char* gb_ = wqw + (size_t)s_ * (64 * 8192) + (e) * (8 * 8192);                            \
      dma4<false>(dst_, vWq[e][0], vWq[e][1], gb_, gb_ + 4 * 8192);                                   \
      dma1(L0 + AUX + ((i) & 3) * 8192 + (j2 + (e)) * 1024, vX[e], xw + s_ * 128 + (e) * (8 * 896));  \
    } else if ((i) < 15) {                                                                            \
      const char* gb_ = kw + (((i) - 7) ^ swp) * (64 * 512)        + (e) * (16 * 512);                \
      dma4<KV_NT>(dst_, vR[e][0], vR[e][1], gb_, gb_ + 8 * 512);                                      \
    } else if ((i) < 23) {                                                                            \
      const char* gb_ = vw + (((i) - 15) ^ swp) * (64 * 512)       + (e) * (8 * 512);                 \
      dma4<KV_NT>(dst_, vR[e][0], vR[e][1], gb_, gb_ + 4 * 512);                                      \
    } else {                                                                                          \
      const char* gb_ = wow + ROT7((unsigned)((i) - 23)) * 128 + (e) * (32 * 896);                    \
      dma4<false>(dst_, vWo[0], vWo[1], gb_, gb_ + 16 * 896);                                         \
    }                                                                                                 \
  } while (0)
#define FEED(i, n) do { HALF(i, 1); HALF((i) + 1, 0); WAIT_V(n); BAR(); } while (0)
#define FEED_MID(i, n) do { HALF(i, 1); BAR(); HALF((i) + 1, 0); WAIT_V(n); BAR(); } while (0)
    HALF(0, 0); HALF(0, 1); HALF(1, 0);
    FEED(1, 15);
    FEED(2, 15); FEED(3, 15); FEED(4, 15); FEED(5, 15); FEED(6, 14); FEED(7, 12);
    BAR(); FEED(8, 12);
    FEED(9, 12); FEED(10, 12); FEED(11, 12); FEED(12, 12); FEED(13, 12); FEED(14, 12); FEED(15, 12);
    FEED_MID(16, 12);
    FEED(17, 12); FEED(18, 12); FEED(19, 12); FEED(20, 12); FEED(21, 12); FEED(22, 12); FEED(23, 12);
#define WHOLE(i) do { HALF(i, 0); HALF(i, 1); } while (0)
    HALF(24, 1); BAR();
    WAIT_V(0); BAR();
    WHOLE(25); WHOLE(26); WAIT_V(8); BAR();
    WHOLE(27); WAIT_V(8); BAR();
    WHOLE(28); WAIT_V(8); BAR();
    WHOLE(29); WAIT_V(8); BAR();
    WAIT_V(0); BAR();
  } else {
#define SYNC(i) do { if ((i) > 0) STAMP(2 * (i) - 1); WAIT_L0(); BAR(); STAMP(2 * (i)); } while (0)
#define MID() do { WAIT_L0(); BAR(); } while (0)
    f32x4 acc[2][4];
#pragma unroll
    for (int i = 0; i < 2; ++i)
#pragma unroll
      for (int j = 0; j < 4; ++j) acc[i][j] = (f32x4){0.f, 0.f, 0.f, 0.f};
    const unsigned aT = L0 + (8 * g + qq) * 512 + (((2 * w) ^ (qq | ((g & 1) << 2))) * 32) + 8 * p;
    unsigned aX0 = L0 + AUX + r * 128 + 16 * (g ^ (r >> 1)), aX1 = aX0 ^ 64;
    unsigned aLin = L0 + AUX + l * 16;
    OPAQUE(aX0); OPAQUE(aX1); OPAQUE(aLin);
    bf16x8 fa[2][2], fb[2][4];
#define ROWS_READ(i, BADDR)                                                                           \
  do {                                                                                                \
    unsigned a0_ = aT + ((i) & 2) * SLOT, a1_ = (aT ^ 32) + ((i) & 2) * SLOT;                         \
    OPAQUE(a0_); OPAQUE(a1_);                                                                         \
    _Pragma("unroll") for (int u = 0; u < 2; ++u) {                                                   \
      _Pragma("unroll") for (int dt = 0; dt < 2; ++dt) {                                              \
        const unsigned a_ = (dt ? a1_ : a0_) + ((i) & 1) * SLOT + u * 16384;                          \
        fa[u][dt] = lds_tr(a_, a_ + 2048);                                                            \
      }                                                                                               \
      _Pragma("unroll") for (int qt_ = 0; qt_ < 4; ++qt_) fb[u][qt_] = lds_b128(BADDR);               \
    }                                                                                                 \
  } while (0)
#define ROWS_MM()                                                                                     \
  do {                                                                                                \
    _Pragma("unroll") for (int u = 0; u < 2; ++u)                                                     \
        _Pragma("unroll") for (int dt = 0; dt < 2; ++dt)                                              \
            _Pragma("unroll") for (int t = 0; t < 4; ++t) acc[dt][t] = mfma(fa[u][dt], fb[u][t], acc[dt][t]); \
  } while (0)
#define Q_READ(i) ROWS_READ(i, (u ? aX1 : aX0) + ((i) & 3) * 8192 + qt_ * 2048)
    SYNC(0); Q_READ(0); ROWS_MM();
    SYNC(1); Q_READ(1); ROWS_MM();
    SYNC(2); Q_READ(2); ROWS_MM();
    SYNC(3); Q_READ(3); ROWS_MM();
    SYNC(4); Q_READ(4); ROWS_MM();
    SYNC(5); Q_READ(5); ROWS_MM();
    unsigned aFw = L0 + AUX + w * 4096 + ((g >> 1) * 16 + r) * 16 + 8 * (g & 1);
    OPAQUE(aFw);
#define TILE_MM(dt)                                                                                   \
  do {                                                                                                \
    _Pragma("unroll") for (int u = 0; u < 2; ++u)                                                     \
        _Pragma("unroll") for (int t = 0; t < 4; ++t) acc[dt][t] = mfma(fa[u][dt], fb[u][t], acc[dt][t]); \
  } while (0)
#define TILE_WRITE(dt)                                                                                \
  do {                                                                                                \
    _Pragma("unroll") for (int t = 0; t < 4; ++t) {                                                   \
      u32x2 v = {pack2(acc[dt][t][0], acc[dt][t][1]), pack2(acc[dt][t][2], acc[dt][t][3])};           \
      *(LAS u32x2*)(aFw + t * 1024 + dt * 512) = v;                                                   \
    }                                                                                                 \
  } while (0)
#define HAND_OFF() do { PIN(); TILE_MM(0); PIN(); TILE_MM(1); TILE_WRITE(0); PIN(); TILE_WRITE(1); } while (0)
    SYNC(6); Q_READ(6); MID(); HAND_OFF();

    constexpr float CEXP = 1.4426950408889634f / 16.0f;
    const unsigned kh = w & 1, qt = w >> 1, qh = w >> 2, cq = w & 3;
    f32x4 sc[8][2];
    float mrow, lsum = 0.f, linv[2];
    unsigned aOwn = aLin + qt * 1024, aSr = L0 + STAT + r * 8, aSw = aSr + qt * 128;
    OPAQUE(aOwn); OPAQUE(aSr); OPAQUE(aSw);
    {
      bf16x8 bq[8];
      const unsigned aK = L0 + (32 * kh + r) * 512 + 16 * (g ^ ((r & 8) | ((r & 3) << 1)));
#define K_READ(i, c)                                                                                  \
  do {                                                                                                \
    unsigned a_ = (aK ^ ((c) * 64)) + ((i) & 2) * SLOT;                                               \
    OPAQUE(a_);                                                                                       \
    _Pragma("unroll") for (int v = 0; v < 2; ++v)                                                     \
        _Pragma("unroll") for (int kt = 0; kt < 2; ++kt)                                              \
            ka[(c) & 1][v][kt] = lds_b128(a_ + ((i) & 1) * SLOT + kt * 8192 + v * 256);               \
  } while (0)
#define S_MM(i, c)                                                                                    \
  do {                                                                                                \
    _Pragma("unroll") for (int v = 0; v < 2; ++v)                                                     \
        _Pragma("unroll") for (int kt = 0; kt < 2; ++kt)                                              \
            sc[(i) - 7][kt] = mfma(ka[(c) & 1][v][kt], bq[(c) + 4 * v], sc[(i) - 7][kt]);             \
  } while (0)
#define MAX8(j) fmaxf(fmaxf(fmaxf(sc[j][0][0], sc[j][0][1]), fmaxf(sc[j][0][2], sc[j][0][3])),        \
                      fmaxf(fmaxf(sc[j][1][0], sc[j][1][1]), fmaxf(sc[j][1][2], sc[j][1][3])))
#define S_STEP(i)                                                                                     \
  do {                                                                                                \
    bf16x8 ka[2][2][2];                                                                               \
    sc[(i) - 7][0] = sc[(i) - 7][1] = (f32x4){0.f, 0.f, 0.f, 0.f};                                    \
    K_READ(i, 0); K_READ(i, 1); PIN();                                                                \
    if ((i) == 8) mrow = MAX8(0);                                                                     \
    if ((i) > 8) mrow = fmaxf(mrow, MAX8((i) > 8 ? (i) - 8 : 0));                                     \
    if ((i) >= 8) OPAQUE(mrow);                                                                       \
    S_MM(i, 0); PIN(); K_READ(i, 2); S_MM(i, 1); PIN(); K_READ(i, 3); S_MM(i, 2); PIN(); S_MM(i, 3);  \
  } while (0)
      SYNC(7);
#pragma unroll
      for (int ks = 0; ks < 8; ++ks) bq[ks] = lds_b128(aOwn + ks * 4096);
      S_STEP(7);
      SYNC(8);  S_STEP(8);
      SYNC(9);  S_STEP(9);
      SYNC(10); S_STEP(10);
      SYNC(11); S_STEP(11);
      SYNC(12); S_STEP(12);
      SYNC(13); S_STEP(13);
      SYNC(14); S_STEP(14);
    }
    *(LAS float*)(aSw + kh * 4) = xg_max(fmaxf(mrow, MAX8(7)));
    MID();
    {
      const f32x2 mm = *(LAS f32x2*)(aSw);
      mrow = fmaxf(mm[0], mm[1]) * CEXP;
    }

#define P_WRITE(j)                                                                                    \
  do {                                                                                                \
    f32x4 e_[2];                                                                                      \
    _Pragma("unroll") for (int t = 0; t < 2; ++t)                                                     \
        _Pragma("unroll") for (int c = 0; c < 4; ++c) e_[t][c] = __builtin_amdgcn_exp2f(fmaf(sc[j][t][c], CEXP, -mrow)); \
    lsum += ((e_[0][0] + e_[0][1]) + (e_[0][2] + e_[0][3])) + ((e_[1][0] + e_[1][1]) + (e_[1][2] + e_[1][3])); \
    OPAQUE(lsum);                                                                                     \
    u32x4 v = {pack2(e_[0][0], e_[0][1]), pack2(e_[0][2], e_[0][3]), pack2(e_[1][0], e_[1][1]), pack2(e_[1][2], e_[1][3])}; \
    *(LAS u32x4*)(aOwn + kh * 4096 + ((j) & 1) * 8192) = v;                                           \
  } while (0)
#define O_READ(i) ROWS_READ(i, aLin + (((i) - 15) & 1) * 8192 + (u * 4 + qt_) * 1024)
#define O_STEP(i) do { O_READ(i); P_WRITE((i) - 14); ROWS_MM(); } while (0)
    P_WRITE(0);
#pragma unroll
    for (int i = 0; i < 2; ++i)
#pragma unroll
      for (int j = 0; j < 4; ++j) acc[i][j] = (f32x4){0.f, 0.f, 0.f, 0.f};
    SYNC(15); O_STEP(15);
    SYNC(16); O_STEP(16);
    SYNC(17); O_STEP(17);
    SYNC(18); O_STEP(18);
    SYNC(19); O_STEP(19);
    SYNC(20); O_STEP(20);
    SYNC(21); O_READ(21); P_WRITE(7);
    *(LAS float*)(aSw + (SUMS - STAT) + kh * 4) = xg_sum(lsum);
    ROWS_MM();
    SYNC(22);
    {
      f32x2 ss[2];
      O_READ(22);
#pragma unroll
      for (int t = 0; t < 2; ++t) ss[t] = *(LAS f32x2*)(aSr + (SUMS - STAT) + (2 * qh + t) * 128);
      MID();
      HAND_OFF();
#pragma unroll
      for (int t = 0; t < 2; ++t) linv[t] = __builtin_amdgcn_rcpf(ss[t][0] + ss[t][1]);
    }

    const unsigned pOff = h * PART_BYTES + (qh * 8 + (r >> 1)) * 3584 + cq * 128 + g * 32 + (r & 1) * 16;
    unsigned aWo[2];
    aWo[0] = L0 + (8 * g + qq) * 128 + ((cq ^ (((qq >> 1) & 1) | ((g & 1) << 1))) * 32) + 8 * p;
    aWo[1] = aWo[0] + 2 * SLOT;
    unsigned aB = aLin + qh * 2048;
    OPAQUE(aWo[0]); OPAQUE(aWo[1]); OPAQUE(aB);
    bf16x8 bf[2][8], wa[2][8];
    f32x4 po[2][2];
    u32x4 pk[7];
#define W_FRAG(i, ks) lds_tr(aWo[((i) >> 1) & 1] + ((i) & 1) * SLOT + (ks) * 4096, aWo[((i) >> 1) & 1] + ((i) & 1) * SLOT + (ks) * 4096 + 512)
#define PACK(i)                                                                                       \
  do {                                                                                                \
    const f32x4 a_ = po[(i) & 1][0] * linv[0], b_ = po[(i) & 1][1] * linv[1];                         \
    pk[(i) - 23] = (u32x4){pack2(a_[0], a_[1]), pack2(a_[2], a_[3]), pack2(b_[0], b_[1]), pack2(b_[2], b_[3])}; \
  } while (0)
#define PUBLISH(i) __builtin_amdgcn_raw_buffer_store_b128(pk[(i) - 23], prs, pOff, ROT7((unsigned)((i) - 23)) * 512, 16)
#define W_STEP(i)                                                                                     \
  do {                                                                                                \
    po[(i) & 1][0] = po[(i) & 1][1] = (f32x4){0.f, 0.f, 0.f, 0.f};                                    \
    _Pragma("unroll") for (int ks = 0; ks < 8; ++ks) {                                                \
      PIN();                                                                                          \
      po[(i) & 1][0] = mfma(wa[(i) & 1][ks], bf[0][ks], po[(i) & 1][0]);                              \
      po[(i) & 1][1] = mfma(wa[(i) & 1][ks], bf[1][ks], po[(i) & 1][1]);                              \
      if ((i) > 23 && (i) < 29 && ks < 4) {                                                           \
        wa[((i) + 1) & 1][2 * ks] = W_FRAG((i) + 1, 2 * ks);                                          \
        wa[((i) + 1) & 1][2 * ks + 1] = W_FRAG((i) + 1, 2 * ks + 1);                                  \
      }                                                                                               \
      if ((i) > 23 && ks == 1) PACK((i) - 1);                                                         \
      if ((i) > 23 && ks == 7) PUBLISH((i) - 1);                                                      \
    }                                                                                                 \
    PIN();                                                                                            \
  } while (0)
#define FIRST_READ(ks)                                                                                \
  do {                                                                                                \
    wa[1][ks] = W_FRAG(23, ks);                                                                       \
    bf[0][ks] = lds_b128(aB + (ks) * 4096);                                                           \
    bf[1][ks] = lds_b128(aB + (ks) * 4096 + 1024);                                                    \
  } while (0)
    SYNC(23);
    FIRST_READ(0); FIRST_READ(1); FIRST_READ(2);
    po[1][0] = po[1][1] = (f32x4){0.f, 0.f, 0.f, 0.f};
#pragma unroll
    for (int ks = 0; ks < 8; ++ks) {
      PIN();
      po[1][0] = mfma(wa[1][ks], bf[0][ks], po[1][0]);
      po[1][1] = mfma(wa[1][ks], bf[1][ks], po[1][1]);
      PIN();
      if (ks < 5) FIRST_READ(ks + 3);
      if (ks >= 4) {
        wa[0][2 * ks - 8] = W_FRAG(24, 2 * ks - 8);
        wa[0][2 * ks - 7] = W_FRAG(24, 2 * ks - 7);
      }
    }
    PIN();
    SYNC(24); W_STEP(24);
    SYNC(25); W_STEP(25);
    SYNC(26); W_STEP(26);
    SYNC(27); W_STEP(27);
    SYNC(28); W_STEP(28);
    STAMP(57); STAMP(58);
    W_STEP(29);
    PACK(29); PUBLISH(29);
    STAMP(59);
  }

  WAIT_V(0); BAR();
  STAMP(60);
  if (w == 0) {
    gu32* peer = (gu32*)(ws + FLAG_OFF + (grp * 16 + (l & 15)) * FLAG_STRIDE) + h;
    unsigned spins = 0;
    for (;;) {
      if ((spins & 63) == 0 && l < 16) __hip_atomic_store(peer, 1u, RLX_AGENT);
      const unsigned f = __hip_atomic_load(myflags + (l & 15), RLX_AGENT);
      if (__all(f == 1u)) break;
      if (++spins > (1u << 22)) break;
      __builtin_amdgcn_s_sleep(1);
    }
    if (l < 16) __hip_atomic_store(peer, 1u, RLX_AGENT);
  }
  BAR();
  STAMP(61);
  __builtin_amdgcn_fence(__ATOMIC_ACQUIRE, "wavefront");
  if (w < 7) {
    const unsigned half = l >> 5, it = l & 31;
    const unsigned off = half * (8 * PART_BYTES) + h * 3584 + w * 512 + it * 16;
    u32x4 pv[8];
#pragma unroll
    for (int e = 0; e < 8; ++e) pv[e] = __builtin_amdgcn_raw_buffer_load_b128(prs, off + e * PART_BYTES, 0, 16);
    float s[8];
#pragma unroll
    for (int i = 0; i < 8; ++i) s[i] = 0.f;
#pragma unroll
    for (int e = 0; e < 8; ++e)
#pragma unroll
      for (int i = 0; i < 4; ++i) {
        s[2 * i] += __uint_as_float(pv[e][i] << 16);
        s[2 * i + 1] += __uint_as_float(pv[e][i] & 0xffff0000u);
      }
    float o[4];
#pragma unroll
    for (int i = 0; i < 4; ++i) {
      auto sw = __builtin_amdgcn_permlane32_swap(__float_as_uint(s[i]), __float_as_uint(s[4 + i]), false, false);
      o[i] = __uint_as_float(sw[0]) + __uint_as_float(sw[1]);
    }
    const unsigned q = 32 * (h >> 3) + 16 * half + 2 * (h & 7) + (it & 1);
    const unsigned n = 64 * w + 4 * (it >> 1);
    u32x2 v = {pack2(o[0], o[1]), pack2(o[2], o[3])};
    *(u32x2*)(out + (size_t)(b * 64 + q) * 1792 + br * 896 + n * 2) = v;
  }
}

extern "C" void kernel_launch(void* const* d_in, const int* in_sizes, int n_in, void* d_out, int out_size, void* d_ws,
                              size_t ws_size, hipStream_t stream) {
  gqa2_fused<<<256, 768, 0, stream>>>((const char*)d_in[0], (const char*)d_in[1], (const char*)d_in[2], (const char*)d_in[3],
                                      (const char*)d_in[4], (const char*)d_in[5], (const char*)d_in[6], (const char*)d_in[7],
                                      (const char*)d_in[8], (const char*)d_in[9], (char*)d_out, (char*)d_ws);
}
```
